```python
import torch

def _softmax(x, axis=-1):
    return torch.softmax(x, dim=axis)
def _gelu(x):
    return torch.nn.functional.gelu(x, approximate="tanh")
def _silu(x):
    return torch.nn.functional.silu(x)
def _sigmoid(x):
    return torch.sigmoid(x)
def _layernorm(x, axis=-1, eps=1e-5):
    m = x.mean(dim=axis, keepdim=True)
    v = x.var(dim=axis, keepdim=True, unbiased=False)
    return (x - m) / torch.sqrt(v + eps)
def _scatter_add(tgt, idx, src, axis=0):
    return tgt.clone().index_add_(axis, idx, src)
def _segment_sum(src, seg, n):
    out = torch.zeros((n,) + src.shape[1:], dtype=src.dtype)
    return out.index_add_(0, seg, src)
def _pad(x, pw):
    flat = tuple(v for pair in reversed(pw) for v in pair)
    return torch.nn.functional.pad(x, flat)

kernel_name = 'gated_mix__mha_block__moe_arch_mod__prompt'

def setup_inputs():

    _w = (lambda a: (lambda t: t.to(torch.bfloat16) if t.is_floating_point() else t)(torch.as_tensor(a)))
    def _A_setup():
        B, S, D, H = 1, 2048, 512, 32
        return {
            "x": _w((lambda *s: torch.randn(*s, dtype=torch.bfloat16) * 0.05)(B, S, D)),
            "wqkv": _w((lambda *s: torch.randn(*s, dtype=torch.bfloat16) * 0.8)(D, 3 * D)),
            "wo": _w((lambda *s: torch.randn(*s, dtype=torch.bfloat16) * 0.1)(D, D)),
        }
    _A_in = _A_setup()

    def _B_setup():
        B, S, D, F, C = 1, 2048, 512, 1376, 1
        return {
            "x": _w((lambda *s: torch.randn(*s, dtype=torch.bfloat16) * 0.05)(B, S, D)),
            "w_r": _w((lambda *s: torch.randn(*s, dtype=torch.bfloat16) * 3.2)(D)),
            "w_gate": _w((lambda *s: torch.randn(*s, dtype=torch.bfloat16) * 0.2)(D, F)),
            "w_up": _w((lambda *s: torch.randn(*s, dtype=torch.bfloat16) * 0.2)(D, F)),
            "w_down": _w((lambda *s: torch.randn(*s, dtype=torch.bfloat16) * 0.2)(F, D)),
        }
    _B_in = _B_setup()

    return {
        "A_x": _A_in["x"],
        "A_wqkv": _A_in["wqkv"],
        "A_wo": _A_in["wo"],
        "B_x": _B_in["x"],
        "B_w_r": _B_in["w_r"],
        "B_w_gate": _B_in["w_gate"],
        "B_w_up": _B_in["w_up"],
        "B_w_down": _B_in["w_down"],
        "gate_w": _w((lambda *s: torch.randn(*s, dtype=torch.bfloat16) * 0.05)(512) * 16.0),
    }

def reference(A_x, A_wqkv, A_wo, B_x, B_w_r, B_w_gate, B_w_up, B_w_down, gate_w):
    def _A_ref(x, wqkv, wo):
        B, S, D = x.shape
        H = 32
        Dh = D // H
        qkv = (lambda x, *d: x.permute(*d))((x @ wqkv).reshape(B, S, 3, H, Dh), 2, 0, 3, 1, 4)
        q, k, v = qkv[0], qkv[1], qkv[2]
        scores = torch.einsum("bhqd,bhkd->bhqk", q, k) * (Dh ** -0.5)
        p = _softmax(scores, axis=-1)
        o = torch.einsum("bhqk,bhkd->bhqd", p, v)
        return (lambda x, *d: x.permute(*d))(o, 0, 2, 1, 3).reshape(B, S, D) @ wo

    def _B_ref(x, w_r, w_gate, w_up, w_down):
        B, S, D = x.shape
        C = 1
        r = x @ w_r
        idx = torch.argsort(-r, axis=-1)[:, :C]
        idx_e = (lambda x, n, axis: torch.repeat_interleave(x, n, dim=axis))(idx.reshape(B, C, 1), D, axis=2)
        x_sel = (lambda x, i, axis: torch.take_along_dim(x, i, dim=axis))(x, idx_e, axis=1)
        g = _silu(x_sel @ w_gate)
        u = x_sel @ w_up
        y = (g * u) @ w_down
        r_sel = _sigmoid((lambda x, i, axis: torch.take_along_dim(x, i, dim=axis))(r, idx, axis=1))
        y = y * r_sel.reshape(B, C, 1)
        b_off = (torch.arange(B) * S).reshape(B, 1)
        flat_idx = (idx + b_off).reshape(B * C)
        zero = (x * 0.0).reshape(B * S, D)
        upd = _scatter_add(zero, flat_idx, y.reshape(B * C, D), axis=0)
        return x + upd.reshape(B, S, D)

    x = A_x
    g = _sigmoid((x * gate_w).sum(-1, keepdims=True))
    a = _A_ref(x, A_wqkv, A_wo)
    b = _B_ref(x, B_w_r, B_w_gate, B_w_up, B_w_down)
    return g * a + (1 - g) * b
```

```cpp
#include <hip/hip_runtime.h>
#include <hip/hip_bf16.h>
#include <stdint.h>

typedef __bf16 bf16x8 __attribute__((ext_vector_type(8)));
typedef __bf16 bf16x2 __attribute__((ext_vector_type(2)));
typedef short s16x4 __attribute__((ext_vector_type(4)));
typedef short s16x8 __attribute__((ext_vector_type(8)));
typedef float f32x16 __attribute__((ext_vector_type(16)));
typedef float f32x4 __attribute__((ext_vector_type(4)));
typedef float f32x2 __attribute__((ext_vector_type(2)));
typedef unsigned u32x4 __attribute__((ext_vector_type(4)));
typedef unsigned short u16;
#define LDSP __attribute__((address_space(3)))
typedef LDSP const char* lds_cptr;
typedef LDSP char* lds_ptr;
#define DI __device__ __forceinline__
#define MFMA32(a, b, c) __builtin_amdgcn_mfma_f32_32x32x16_bf16(a, b, c, 0, 0, 0)
#define SBAR() __builtin_amdgcn_sched_barrier(0)

constexpr int SEQ = 2048, DM = 512, NH = 32, FF = 1376;
constexpr float QSCALE = 0.25f * 1.4426950408889634f;

constexpr size_t WS_Q = 0, WS_K = 2u << 20, WS_V = 4u << 20, WS_O = 6u << 20;
constexpr size_t WS_GATE = 8u << 20;
constexpr size_t WS_KEYS = WS_GATE + 8192;
constexpr size_t WS_H = WS_KEYS + 1024;
constexpr size_t WS_YP = WS_H + 8192;

DI unsigned cvtpk(float lo, float hi) { f32x2 v = {lo, hi}; bf16x2 b = __builtin_convertvector(v, bf16x2); return __builtin_bit_cast(unsigned, b); }
DI float bf2f(unsigned b) { return __uint_as_float(b << 16); }
DI float rbf(float x) { return __uint_as_float(cvtpk(x, 0.f) << 16); }
DI unsigned lds_addr(const void* p) { return (unsigned)(size_t)(LDSP const char*)p; }
DI void glds16(unsigned voff, const void* sbase, unsigned ldsdst) {
    asm volatile("s_mov_b32 m0, %2\n\ts_nop 0\n\tglobal_load_lds_dwordx4 %0, %1" ::"v"(voff), "s"(sbase), "s"(ldsdst) : "memory");
}
DI bf16x8 lds_rd128(lds_cptr p) { return *(LDSP const bf16x8*)p; }
DI s16x4 lds_tr(lds_cptr p) { return __builtin_bit_cast(s16x4, __builtin_amdgcn_ds_read_tr16_b64_v4i16((LDSP s16x4*)p)); }
DI bf16x8 lds_tr2(lds_cptr p) {
    s16x4 a = lds_tr(p), b = lds_tr(p + 256);
    s16x8 r = {a[0], a[1], a[2], a[3], b[0], b[1], b[2], b[3]};
    return __builtin_bit_cast(bf16x8, r);
}
#define WAIT_BAR(N) asm volatile("s_waitcnt vmcnt(" #N ") lgkmcnt(0)\n\ts_barrier" ::: "memory")
DI int swap23(int v) { return (v & ~12) | ((v & 4) << 1) | ((v & 8) >> 1); }

constexpr int K1_SLOT = 28672, K1_NSLOT = 5, K1_VEC = K1_SLOT * K1_NSLOT, K1_LDS = K1_VEC + 2048;

template <bool VEC>
DI void k1_stage(lds_cptr sl, lds_cptr vecp, const unsigned (&xo)[4], unsigned wq, unsigned wv, int s, f32x16& aq, f32x16& ak, f32x16& av, f32x16& ar) {
#pragma unroll
    for (int ks = 0; ks < 4; ++ks) {
        bf16x8 xf = lds_rd128(sl + xo[ks]);
        bf16x8 fq = lds_tr2(sl + 16384 + ks * 1024 + wq);
        bf16x8 fk = lds_tr2(sl + 16384 + 4096 + ks * 1024 + wq);
        bf16x8 fv = lds_tr2(sl + 16384 + 8192 + ks * 1024 + wv);
        aq = MFMA32(fq, xf, aq);
        ak = MFMA32(fk, xf, ak);
        av = MFMA32(xf, fv, av);
        if (VEC) { bf16x8 fr = lds_rd128(vecp + s * 128 + ks * 32); ar = MFMA32(fr, xf, ar); }
    }
}

template <bool VEC>
DI void k1_body(char* lds, const u16* x, const u16* wqkv, const u16* w_r, const u16* gate_w, char* ws, int ti, int tj) {
    const int tid = threadIdx.x, lane = tid & 63, w = __builtin_amdgcn_readfirstlane(tid >> 6);
    const int r = lane & 31, h = lane >> 5;
    const unsigned lds0 = lds_addr(lds);
    const unsigned xv0 = (lane >> 3) * 1024 + ((lane & 7) ^ ((lane >> 4) & 7)) * 16;
    const unsigned xv1 = (lane >> 3) * 1024 + ((lane & 7) ^ (((lane >> 4) + 4) & 7)) * 16;
    const unsigned wvo = (lane >> 2) * 3072 + (lane & 3) * 16;
    const char* xb = (const char*)x + (size_t)(128 * ti + 32 * w) * 1024;
    const char* wb = (const char*)wqkv + tj * 64;
    auto issue = [&](int s) {
        const unsigned sl = lds0 + (s % K1_NSLOT) * K1_SLOT;
#pragma unroll
        for (int q = 0; q < 4; ++q) glds16((q & 1) ? xv1 : xv0, xb + q * 8192 + s * 128, sl + (4 * w + q) * 1024);
#pragma unroll
        for (int q = 0; q < 3; ++q) { const int qq = 3 * w + q; glds16(wvo, wb + (size_t)(64 * s + 16 * (qq & 3)) * 3072 + (qq >> 2) * 1024, sl + 16384 + qq * 1024); }
    };
    if (VEC && w < 2) glds16(lane * 16, w == 0 ? w_r : gate_w, lds0 + K1_VEC + w * 1024);
#pragma unroll
    for (int s = 0; s < 5; ++s) issue(s);
    unsigned xo[4];
#pragma unroll
    for (int ks = 0; ks < 4; ++ks) xo[ks] = (32 * w + r) * 128 + ((2 * ks + h) ^ ((r >> 1) & 7)) * 16;
    const int blk = (lane >> 4) & 1, q4 = (lane & 15) >> 2, p4 = lane & 3;
    const unsigned wv = (8 * h + q4) * 64 + blk * 32 + p4 * 8;
    const unsigned wq = (8 * h + q4) * 64 + blk * 32 + (((p4 & 1) << 1) | (p4 >> 1)) * 8;
    lds_cptr L = (lds_cptr)lds;
    lds_cptr vecp = L + K1_VEC + (r & 1) * 1024 + h * 16;
    f32x16 aq = {}, ak = {}, av = {}, ar = {};
#define K1_STEP(s, N) WAIT_BAR(N); if ((s) >= 1 && (s) <= 3) issue((s) + 4); k1_stage<VEC>(L + ((s) % K1_NSLOT) * K1_SLOT, vecp, xo, wq, wv, s, aq, ak, av, ar); SBAR();
    K1_STEP(0, 28) K1_STEP(1, 21) K1_STEP(2, 21) K1_STEP(3, 21) K1_STEP(4, 21) K1_STEP(5, 14) K1_STEP(6, 7) K1_STEP(7, 0)
#undef K1_STEP
    const int tile = 4 * ti + w;
    {
        char* qd = ws + WS_Q + (size_t)(2 * tj) * 65536 + tile * 1024 + lane * 16;
        char* kd = ws + WS_K + (size_t)(2 * tj) * 65536 + tile * 1024 + lane * 16;
#pragma unroll
        for (int e = 0; e < 2; ++e) {
            u32x4 a, b;
#pragma unroll
            for (int i = 0; i < 4; ++i) { a[i] = cvtpk(aq[8 * e + 2 * i] * QSCALE, aq[8 * e + 2 * i + 1] * QSCALE); b[i] = cvtpk(ak[8 * e + 2 * i], ak[8 * e + 2 * i + 1]); }
            *(u32x4*)(qd + e * 65536) = a;
            *(u32x4*)(kd + e * 65536) = b;
        }
        char* vd = ws + WS_V + (size_t)(2 * tj + (r >> 4)) * 65536 + tile * 1024 + h * 256 + swap23(r & 15) * 16;
#pragma unroll
        for (int s = 0; s < 2; ++s) {
            u32x4 a;
#pragma unroll
            for (int i = 0; i < 4; ++i) a[i] = cvtpk(av[8 * s + 2 * i], av[8 * s + 2 * i + 1]);
            *(u32x4*)(vd + s * 512) = a;
        }
    }
    if (VEC) {
        const int token = 128 * ti + 32 * w + r;
        const unsigned rb = cvtpk(ar[0], 0.f) & 0xFFFFu;
        unsigned key = (((rb & 0x8000u) ? (~rb & 0xFFFFu) : (rb | 0x8000u)) << 16) | (0xFFFFu - token);
#pragma unroll
        for (int o = 16; o >= 1; o >>= 1) { const unsigned t = __shfl_xor(key, o); key = t > key ? t : key; }
        if (lane == 0) ((unsigned*)(ws + WS_KEYS))[tile] = key;
        const float gs = rbf(ar[1]);
        const float g = rbf(1.f / (1.f + __expf(-gs)));
        if (lane < 32) ((float*)(ws + WS_GATE))[token] = g;
    }
}

__global__ __launch_bounds__(256) void k1_qkv(const u16* __restrict__ x, const u16* __restrict__ wqkv, const u16* __restrict__ w_r, const u16* __restrict__ gate_w, char* __restrict__ ws) {
    __shared__ __attribute__((aligned(16))) char lds[K1_LDS];
    const int b = blockIdx.x, xcd = b & 7, loc = b >> 3;
    const int ti = (xcd >> 1) * 4 + (loc & 3), tj = (xcd & 1) * 8 + (loc >> 2);
    if (tj == 0) k1_body<true>(lds, x, wqkv, w_r, gate_w, ws, ti, tj);
    else k1_body<false>(lds, x, wqkv, w_r, gate_w, ws, ti, tj);
}

constexpr int K2_K = 0, K2_V = 65536, K2_ONES = 131072, K2_LDS = K2_ONES + 5120;

DI float max16(const f32x16& s) {
    float a = fmaxf(fmaxf(s[0], s[1]), s[2]), b = fmaxf(fmaxf(s[3], s[4]), s[5]);
    a = fmaxf(fmaxf(a, s[6]), s[7]); b = fmaxf(fmaxf(b, s[8]), s[9]);
    a = fmaxf(fmaxf(a, s[10]), s[11]); b = fmaxf(fmaxf(b, s[12]), s[13]);
    a = fmaxf(fmaxf(a, s[14]), s[15]);
    float m = fmaxf(a, b);
    auto rr = __builtin_amdgcn_permlane32_swap(__float_as_uint(m), __float_as_uint(m), false, false);
    return fmaxf(__uint_as_float(rr[0]), __uint_as_float(rr[1]));
}
DI void exp_pack(const f32x16& s, bf16x8& p0, bf16x8& p1) {
    u32x4 a, b;
#pragma unroll
    for (int i = 0; i < 4; ++i) a[i] = cvtpk(__builtin_amdgcn_exp2f(s[2 * i]), __builtin_amdgcn_exp2f(s[2 * i + 1]));
#pragma unroll
    for (int i = 0; i < 4; ++i) b[i] = cvtpk(__builtin_amdgcn_exp2f(s[8 + 2 * i]), __builtin_amdgcn_exp2f(s[8 + 2 * i + 1]));
    p0 = __builtin_bit_cast(bf16x8, a); p1 = __builtin_bit_cast(bf16x8, b);
}

__global__ __launch_bounds__(512) void k2_attn(char* __restrict__ ws) {
    __shared__ __attribute__((aligned(16))) char lds[K2_LDS];
    const int tid = threadIdx.x, lane = tid & 63, w = __builtin_amdgcn_readfirstlane(tid >> 6);
    const int b = blockIdx.x, xcd = b & 7, loc = b >> 3, head = xcd * 4 + (loc >> 3), chunk = loc & 7;
    const int r = lane & 31, h = lane >> 5;
    const unsigned lds0 = lds_addr(lds);
    const char* kb = ws + WS_K + (size_t)head * 65536 + w * 1024;
    const char* vb = ws + WS_V + (size_t)head * 65536 + w * 1024;
    const unsigned vo = lane * 16;
#pragma unroll
    for (int c = 0; c < 8; ++c) { glds16(vo, kb + c * 8192, lds0 + K2_K + c * 8192 + w * 1024); glds16(vo, vb + c * 8192, lds0 + K2_V + c * 8192 + w * 1024); }
    const bf16x8 qf = *(const bf16x8*)(ws + WS_Q + (size_t)head * 65536 + (8 * chunk + w) * 1024 + lane * 16);
    { u32x4 ones = {0x3F803F80u, 0x3F803F80u, 0x3F803F80u, 0x3F803F80u}; if (tid < 320) *(LDSP u32x4*)((lds_ptr)lds + K2_ONES + tid * 16) = ones; }
    lds_cptr L = (lds_cptr)lds;
    lds_cptr kp = L + K2_K + lane * 16;
    const bool isv = r < 16;
    lds_cptr vp = isv ? L + K2_V + h * 256 + r * 16 : L + K2_ONES + ((r >= 20 && r < 28) ? 64 : 0);
    const unsigned vinc = isv ? 4096u : 0u;
    WAIT_BAR(14);
    f32x16 zero = {};
    bf16x8 kf = lds_rd128(kp);
    f32x16 s0 = MFMA32(kf, qf, zero);
    const float mref = max16(s0);
    f32x16 cneg;
#pragma unroll
    for (int i = 0; i < 16; ++i) { cneg[i] = -mref; s0[i] -= mref; }
    f32x16 o = {}, s1;
    kf = lds_rd128(kp + 1024);
    bf16x8 v0 = lds_rd128(vp), v1 = lds_rd128(vp + 512);
    u32x4 pa, pb = {0u, 0u, 0u, 0u};
#define EX(S, i) S[i] = __builtin_amdgcn_exp2f(S[i]); SBAR();
#define CV(P, S, i) P[i & 3] = cvtpk(S[2 * (i)], S[2 * (i) + 1]); SBAR();
#define STEP(SC, SN, u) \
        SN = MFMA32(kf, qf, cneg); SBAR(); \
        kf = lds_rd128(kq + ((u) + 2) * 1024); SBAR(); \
        EX(SC, 0) EX(SC, 1) EX(SC, 2) EX(SC, 3) \
        o = MFMA32(v1, __builtin_bit_cast(bf16x8, pb), o); SBAR(); \
        v1 = lds_rd128(vp + (u) * 1024 + 512); SBAR(); \
        CV(pa, SC, 0) EX(SC, 4) EX(SC, 5) CV(pa, SC, 1) EX(SC, 6) EX(SC, 7) CV(pa, SC, 2) EX(SC, 8) EX(SC, 9) CV(pa, SC, 3) EX(SC, 10) EX(SC, 11) \
        o = MFMA32(v0, __builtin_bit_cast(bf16x8, pa), o); SBAR(); \
        v0 = lds_rd128(vp + ((u) + 1) * 1024); SBAR(); \
        CV(pb, SC, 4) EX(SC, 12) EX(SC, 13) CV(pb, SC, 5) EX(SC, 14) EX(SC, 15) CV(pb, SC, 6) CV(pb, SC, 7)
    for (int it = 0; it < 16; ++it) {
        if (it == 1) WAIT_BAR(12);
        if (it == 3) WAIT_BAR(0);
        lds_cptr kq = kp + it * 4096;
        SBAR();
        STEP(s0, s1, 0) STEP(s1, s0, 1) STEP(s0, s1, 2) STEP(s1, s0, 3)
        vp += vinc;
    }
#undef STEP
#undef EX
#undef CV
    o = MFMA32(v1, __builtin_bit_cast(bf16x8, pb), o);
    bf16x8 p0, p1;
    float l = o[8];
    float chk = 0.f;
#pragma unroll
    for (int i = 0; i < 8; ++i) chk += fabsf(o[i]);
    const bool bad = !(l < 1.152921504606847e18f) || !(chk < 3.0e38f);
    if (__any(bad)) {
        float m = -INFINITY;
        o = f32x16{};
        lds_cptr vq = isv ? L + K2_V + h * 256 + r * 16 : L + K2_ONES;
        for (int t = 0; t < 64; ++t) {
            f32x16 s = MFMA32(lds_rd128(kp + t * 1024), qf, zero);
            const float mn = fmaxf(m, max16(s));
            const float f = __builtin_amdgcn_exp2f(m - mn);
            m = mn;
#pragma unroll
            for (int i = 0; i < 16; ++i) { s[i] -= mn; o[i] *= f; }
            exp_pack(s, p0, p1);
            lds_cptr vt = vq + (isv ? t * 1024 : 0);
            o = MFMA32(lds_rd128(vt), p0, o); o = MFMA32(lds_rd128(vt + 512), p1, o);
        }
        l = o[8];
    }
    const float inv = 1.f / l;
    u32x4 ov;
#pragma unroll
    for (int i = 0; i < 4; ++i) ov[i] = cvtpk(o[2 * i] * inv, o[2 * i + 1] * inv);
    *(u32x4*)(ws + WS_O + (size_t)((8 * chunk + w) * 32 + head) * 1024 + lane * 16) = ov;
}

DI unsigned top_key(const unsigned* __restrict__ keys) {
    unsigned m = 0;
#pragma unroll
    for (int i = 0; i < 64; ++i) { const unsigned k = keys[i]; m = k > m ? k : m; }
    return m;
}
__global__ __launch_bounds__(256) void k_ffn(const u16* __restrict__ x, const u16* __restrict__ wg, const u16* __restrict__ wu, const u16* __restrict__ wd, char* __restrict__ ws) {
    __shared__ float red[2][16][16];
    __shared__ float hs[16];
    const int t = threadIdx.x, u = blockIdx.x;
    const unsigned key = top_key((const unsigned*)(ws + WS_KEYS));
    const int idx = 0xFFFF - (key & 0xFFFF);
    const int c = t & 15, kg = t >> 4;
    float a = 0.f, bsum = 0.f;
    for (int k = kg * 32; k < kg * 32 + 32; ++k) { const float xv = bf2f(x[idx * DM + k]); a += xv * bf2f(wg[k * FF + 16 * u + c]); bsum += xv * bf2f(wu[k * FF + 16 * u + c]); }
    red[0][kg][c] = a; red[1][kg][c] = bsum;
    __syncthreads();
    if (t < 16) {
        float g = 0.f, uu = 0.f;
        for (int i = 0; i < 16; ++i) { g += red[0][i][t]; uu += red[1][i][t]; }
        g = rbf(g); uu = rbf(uu);
        const float sg = rbf(g / (1.f + __expf(-g)));
        hs[t] = rbf(sg * uu);
    }
    __syncthreads();
    for (int n = t; n < DM; n += 256) {
        float y = 0.f;
        for (int f = 0; f < 16; ++f) y += hs[f] * bf2f(wd[(16 * u + f) * DM + n]);
        ((float*)(ws + WS_YP))[u * DM + n] = y;
    }
}

constexpr int K3_Y = 8 * 16384, K3_LDS = K3_Y + 1024;

__global__ __launch_bounds__(256) void k3_out(const u16* __restrict__ x, const u16* __restrict__ wo, const char* __restrict__ ws, u16* __restrict__ out) {
    __shared__ __attribute__((aligned(16))) char lds[K3_LDS];
    const int tid = threadIdx.x, lane = tid & 63, w = __builtin_amdgcn_readfirstlane(tid >> 6);
    const int b = blockIdx.x, xcd = b & 7, loc = b >> 3, tp = xcd * 4 + (loc & 3), ct = loc >> 2;
    const int r = lane & 31, h = lane >> 5;
    const unsigned lds0 = lds_addr(lds);
    const char* ob = ws + WS_O + (size_t)(2 * tp + (w >> 1)) * 32768 + (w & 1) * 2048;
    const char* wb = (const char*)wo + (size_t)((w & 1) * 32) * 1024 + ct * 128 + (w >> 1) * 64;
    const unsigned ovo = lane * 16, wvo = (lane >> 2) * 1024 + (lane & 3) * 16;
#pragma unroll
    for (int s = 0; s < 8; ++s) {
        const unsigned sl = lds0 + s * 16384;
#pragma unroll
        for (int q = 0; q < 2; ++q) glds16(ovo, ob + s * 4096 + q * 1024, sl + (2 * w + q) * 1024);
#pragma unroll
        for (int q = 0; q < 2; ++q) glds16(wvo, wb + (size_t)(64 * s + 16 * q) * 1024, sl + 8192 + (2 * w + q) * 1024);
    }
    const int tt = w & 1, g = w >> 1;
    const int token = 64 * tp + 32 * tt + r, col = 64 * ct + 32 * g + 8 * h;
    const u32x4 x0 = *(const u32x4*)(x + token * DM + col), x1 = *(const u32x4*)(x + token * DM + col + 16);
    const float gt = ((const float*)(ws + WS_GATE))[token];
    const unsigned key = top_key((const unsigned*)(ws + WS_KEYS));
    const int idx = 0xFFFF - (key & 0xFFFF);
    const bool owner = (idx >> 6) == tp;
    LDSP float* yl = (LDSP float*)((lds_ptr)lds + K3_Y);
    if (owner) {
        const float* yp = (const float*)(ws + WS_YP) + 64 * ct + lane;
        float y = 0.f;
        for (int u = w; u < 86; u += 4) y += yp[u * DM];
        yl[w * 64 + lane] = y;
    }
    const int blk = (lane >> 4) & 1, q4 = (lane & 15) >> 2, p4 = lane & 3;
    const unsigned wq = 8192 + g * 4096 + (8 * h + q4) * 64 + blk * 32 + (((p4 & 1) << 1) | (p4 >> 1)) * 8;
    const unsigned oo = tt * 4096 + lane * 16;
    lds_cptr L = (lds_cptr)lds;
    f32x16 acc = {};
#define K3_STEP(s, N) WAIT_BAR(N); { _Pragma("unroll") for (int ks = 0; ks < 4; ++ks) { bf16x8 of = lds_rd128(L + (s) * 16384 + oo + ks * 1024); bf16x8 wf = lds_tr2(L + (s) * 16384 + wq + ks * 1024); acc = MFMA32(wf, of, acc); } } SBAR();
    K3_STEP(0, 28) K3_STEP(1, 24) K3_STEP(2, 20) K3_STEP(3, 16) K3_STEP(4, 12) K3_STEP(5, 8) K3_STEP(6, 4) K3_STEP(7, 0)
#undef K3_STEP
    const float omg = rbf(1.f - gt);
    float ysel[16];
#pragma unroll
    for (int i = 0; i < 16; ++i) ysel[i] = 0.f;
    const bool sel = token == idx;
    if (owner) {
        const unsigned ob16 = key >> 16, rb = (ob16 & 0x8000u) ? (ob16 & 0x7FFFu) : (~ob16 & 0xFFFFu);
        const float rs = rbf(1.f / (1.f + __expf(-bf2f(rb))));
#pragma unroll
        for (int i = 0; i < 16; ++i) { const int c = 32 * g + 8 * h + (i & 7) + 16 * (i >> 3); const float y = rbf((yl[c] + yl[64 + c]) + (yl[128 + c] + yl[192 + c])); ysel[i] = sel ? rbf(y * rs) : 0.f; }
    }
#pragma unroll
    for (int e = 0; e < 2; ++e) {
        const u32x4 xv = e ? x1 : x0;
        u32x4 ov;
#pragma unroll
        for (int i = 0; i < 4; ++i) {
            float o2[2];
#pragma unroll
            for (int j = 0; j < 2; ++j) {
                const float xx = j ? __uint_as_float(xv[i] & 0xFFFF0000u) : __uint_as_float(xv[i] << 16);
                const float bb = rbf(xx + ysel[8 * e + 2 * i + j]);
                const float a = rbf(acc[8 * e + 2 * i + j]);
                o2[j] = rbf(gt * a) + rbf(omg * bb);
            }
            ov[i] = cvtpk(o2[0], o2[1]);
        }
        *(u32x4*)(out + token * DM + col + 16 * e) = ov;
    }
}

extern "C" void kernel_launch(void* const* d_in, const int* in_sizes, int n_in, void* d_out, int out_size, void* d_ws, size_t ws_size, hipStream_t stream) {
    const u16* x = (const u16*)d_in[0];
    const u16* wqkv = (const u16*)d_in[1];
    const u16* wo = (const u16*)d_in[2];
    const u16* w_r = (const u16*)d_in[4];
    const u16* wg = (const u16*)d_in[5];
    const u16* wu = (const u16*)d_in[6];
    const u16* wd = (const u16*)d_in[7];
    const u16* gate_w = (const u16*)d_in[8];
    char* ws = (char*)d_ws;
    k1_qkv<<<256, 256, 0, stream>>>(x, wqkv, w_r, gate_w, ws);
    k_ffn<<<86, 256, 0, stream>>>(x, wg, wu, wd, ws);
    k2_attn<<<256, 512, 0, stream>>>(ws);
    k3_out<<<256, 256, 0, stream>>>(x, wo, ws, (u16*)d_out);
}
```

```cpp
#include <hip/hip_runtime.h>
#include <hip/hip_bf16.h>
#include <stdint.h>

typedef __bf16 bf16x8 __attribute__((ext_vector_type(8)));
typedef __bf16 bf16x2 __attribute__((ext_vector_type(2)));
typedef short s16x4 __attribute__((ext_vector_type(4)));
typedef short s16x8 __attribute__((ext_vector_type(8)));
typedef float f32x16 __attribute__((ext_vector_type(16)));
typedef float f32x4 __attribute__((ext_vector_type(4)));
typedef float f32x2 __attribute__((ext_vector_type(2)));
typedef unsigned u32x4 __attribute__((ext_vector_type(4)));
typedef unsigned short u16;
#define LDSP __attribute__((address_space(3)))
typedef LDSP const char* lds_cptr;
typedef LDSP char* lds_ptr;
#define DI __device__ __forceinline__
#define MFMA32(a, b, c) __builtin_amdgcn_mfma_f32_32x32x16_bf16(a, b, c, 0, 0, 0)
#define SBAR() __builtin_amdgcn_sched_barrier(0)

#define STAMP(kid, i)
constexpr int SEQ = 2048, DM = 512, NH = 32, FF = 1376;
constexpr float QSCALE = 0.25f * 1.4426950408889634f;

constexpr size_t WS_Q = 0, WS_K = 2u << 20, WS_V = 4u << 20, WS_O = 6u << 20;
constexpr size_t WS_GATE = 8u << 20;
constexpr size_t WS_KEYS = WS_GATE + 8192;
constexpr size_t WS_H = WS_KEYS + 1024;
constexpr size_t WS_YP = WS_H + 8192;

DI unsigned cvtpk(float lo, float hi) { f32x2 v = {lo, hi}; bf16x2 b = __builtin_convertvector(v, bf16x2); return __builtin_bit_cast(unsigned, b); }
DI float bf2f(unsigned b) { return __uint_as_float(b << 16); }
DI float rbf(float x) { return __uint_as_float(cvtpk(x, 0.f) << 16); }
DI unsigned lds_addr(const void* p) { return (unsigned)(size_t)(LDSP const char*)p; }
DI void glds16(unsigned voff, const void* sbase, unsigned ldsdst) {
    asm volatile("s_mov_b32 m0, %2\n\ts_nop 0\n\tglobal_load_lds_dwordx4 %0, %1" ::"v"(voff), "s"(sbase), "s"(ldsdst) : "memory");
}
DI bf16x8 lds_rd128(lds_cptr p) { return *(LDSP const bf16x8*)p; }
DI s16x4 lds_tr(lds_cptr p) { return __builtin_bit_cast(s16x4, __builtin_amdgcn_ds_read_tr16_b64_v4i16((LDSP s16x4*)p)); }
DI bf16x8 lds_tr2(lds_cptr p) {
    s16x4 a = lds_tr(p), b = lds_tr(p + 256);
    s16x8 r = {a[0], a[1], a[2], a[3], b[0], b[1], b[2], b[3]};
    return __builtin_bit_cast(bf16x8, r);
}
#define WAIT_BAR(N) asm volatile("s_waitcnt vmcnt(" #N ") lgkmcnt(0)\n\ts_barrier" ::: "memory")
DI int swap23(int v) { return (v & ~12) | ((v & 4) << 1) | ((v & 8) >> 1); }

constexpr int K1_SLOT = 28672, K1_NSLOT = 5, K1_VEC = K1_SLOT * K1_NSLOT, K1_LDS = K1_VEC + 2048;

template <bool VEC>
DI void k1_stage(lds_cptr sl, lds_cptr vecp, const unsigned (&xo)[4], unsigned wq, unsigned wv, int s, f32x16& aq, f32x16& ak, f32x16& av, f32x16& ar) {
#pragma unroll
    for (int ks = 0; ks < 4; ++ks) {
        bf16x8 xf = lds_rd128(sl + xo[ks]);
        bf16x8 fq = lds_tr2(sl + 16384 + ks * 1024 + wq);
        bf16x8 fk = lds_tr2(sl + 16384 + 4096 + ks * 1024 + wq);
        bf16x8 fv = lds_tr2(sl + 16384 + 8192 + ks * 1024 + wv);
        aq = MFMA32(fq, xf, aq);
        ak = MFMA32(fk, xf, ak);
        av = MFMA32(xf, fv, av);
        if (VEC) { bf16x8 fr = lds_rd128(vecp + s * 128 + ks * 32); ar = MFMA32(fr, xf, ar); }
    }
}

template <bool VEC>
DI void k1_body(char* lds, const u16* x, const u16* wqkv, const u16* w_r, const u16* gate_w, char* ws, int ti, int tj) {
    const int tid = threadIdx.x, lane = tid & 63, w = __builtin_amdgcn_readfirstlane(tid >> 6);
    STAMP(0, 0);
    const int r = lane & 31, h = lane >> 5;
    const unsigned lds0 = lds_addr(lds);
    const unsigned xv0 = (lane >> 3) * 1024 + ((lane & 7) ^ ((lane >> 4) & 7)) * 16;
    const unsigned xv1 = (lane >> 3) * 1024 + ((lane & 7) ^ (((lane >> 4) + 4) & 7)) * 16;
    const unsigned wvo = (lane >> 2) * 3072 + (lane & 3) * 16;
    const char* xb = (const char*)x + (size_t)(128 * ti + 32 * w) * 1024;
    const char* wb = (const char*)wqkv + tj * 64;
    auto issue = [&](int s) {
        const unsigned sl = lds0 + (s % K1_NSLOT) * K1_SLOT;
#pragma unroll
        for (int q = 0; q < 4; ++q) glds16((q & 1) ? xv1 : xv0, xb + q * 8192 + s * 128, sl + (4 * w + q) * 1024);
#pragma unroll
        for (int q = 0; q < 3; ++q) { const int qq = 3 * w + q; glds16(wvo, wb + (size_t)(64 * s + 16 * (qq & 3)) * 3072 + (qq >> 2) * 1024, sl + 16384 + qq * 1024); }
    };
    if (VEC && w < 2) glds16(lane * 16, w == 0 ? w_r : gate_w, lds0 + K1_VEC + w * 1024);
#pragma unroll
    for (int s = 0; s < 5; ++s) issue(s);
    unsigned xo[4];
#pragma unroll
    for (int ks = 0; ks < 4; ++ks) xo[ks] = (32 * w + r) * 128 + ((2 * ks + h) ^ ((r >> 1) & 7)) * 16;
    const int blk = (lane >> 4) & 1, q4 = (lane & 15) >> 2, p4 = lane & 3;
    const unsigned wv = (8 * h + q4) * 64 + blk * 32 + p4 * 8;
    const unsigned wq = (8 * h + q4) * 64 + blk * 32 + (((p4 & 1) << 1) | (p4 >> 1)) * 8;
    lds_cptr L = (lds_cptr)lds;
    lds_cptr vecp = L + K1_VEC + (r & 1) * 1024 + h * 16;
    f32x16 aq = {}, ak = {}, av = {}, ar = {};
#define K1_STEP(s, N) WAIT_BAR(N); if ((s) >= 1 && (s) <= 3) issue((s) + 4); k1_stage<VEC>(L + ((s) % K1_NSLOT) * K1_SLOT, vecp, xo, wq, wv, s, aq, ak, av, ar); SBAR();
    K1_STEP(0, 28) STAMP(0, 1); K1_STEP(1, 21) K1_STEP(2, 21) K1_STEP(3, 21) K1_STEP(4, 21) K1_STEP(5, 14) K1_STEP(6, 7) K1_STEP(7, 0)
    STAMP(0, 2);
#undef K1_STEP
    const int tile = 4 * ti + w;
    {
        char* qd = ws + WS_Q + (size_t)(2 * tj) * 65536 + tile * 1024 + lane * 16;
        char* kd = ws + WS_K + (size_t)(2 * tj) * 65536 + tile * 1024 + lane * 16;
#pragma unroll
        for (int e = 0; e < 2; ++e) {
            u32x4 a, b;
#pragma unroll
            for (int i = 0; i < 4; ++i) { a[i] = cvtpk(aq[8 * e + 2 * i] * QSCALE, aq[8 * e + 2 * i + 1] * QSCALE); b[i] = cvtpk(ak[8 * e + 2 * i], ak[8 * e + 2 * i + 1]); }
            *(u32x4*)(qd + e * 65536) = a;
            *(u32x4*)(kd + e * 65536) = b;
        }
        char* vd = ws + WS_V + (size_t)(2 * tj + (r >> 4)) * 65536 + tile * 1024 + h * 256 + swap23(r & 15) * 16;
#pragma unroll
        for (int s = 0; s < 2; ++s) {
            u32x4 a;
#pragma unroll
            for (int i = 0; i < 4; ++i) a[i] = cvtpk(av[8 * s + 2 * i], av[8 * s + 2 * i + 1]);
            *(u32x4*)(vd + s * 512) = a;
        }
    }
    if (VEC) {
        const int token = 128 * ti + 32 * w + r;
        const unsigned rb = cvtpk(ar[0], 0.f) & 0xFFFFu;
        unsigned key = (((rb & 0x8000u) ? (~rb & 0xFFFFu) : (rb | 0x8000u)) << 16) | (0xFFFFu - token);
#pragma unroll
        for (int o = 16; o >= 1; o >>= 1) { const unsigned t = __shfl_xor(key, o); key = t > key ? t : key; }
        if (lane == 0) ((unsigned*)(ws + WS_KEYS))[tile] = key;
        const float gs = rbf(ar[1]);
        const float g = rbf(1.f / (1.f + __expf(-gs)));
        if (lane < 32) ((float*)(ws + WS_GATE))[token] = g;
    }
    STAMP(0, 3);
}

__global__ __launch_bounds__(256) void k1_qkv(const u16* __restrict__ x, const u16* __restrict__ wqkv, const u16* __restrict__ w_r, const u16* __restrict__ gate_w, char* __restrict__ ws) {
    __shared__ __attribute__((aligned(16))) char lds[K1_LDS];
    const int b = blockIdx.x, xcd = b & 7, loc = b >> 3;
    const int ti = (xcd >> 1) * 4 + (loc & 3), tj = (xcd & 1) * 8 + (loc >> 2);
    if (tj == 0) k1_body<true>(lds, x, wqkv, w_r, gate_w, ws, ti, tj);
    else k1_body<false>(lds, x, wqkv, w_r, gate_w, ws, ti, tj);
}

constexpr int K2_K = 0, K2_V = 65536, K2_ONES = 131072, K2_LDS = K2_ONES + 5120;

DI float max16(const f32x16& s) {
    float a = fmaxf(fmaxf(s[0], s[1]), s[2]), b = fmaxf(fmaxf(s[3], s[4]), s[5]);
    a = fmaxf(fmaxf(a, s[6]), s[7]); b = fmaxf(fmaxf(b, s[8]), s[9]);
    a = fmaxf(fmaxf(a, s[10]), s[11]); b = fmaxf(fmaxf(b, s[12]), s[13]);
    a = fmaxf(fmaxf(a, s[14]), s[15]);
    float m = fmaxf(a, b);
    auto rr = __builtin_amdgcn_permlane32_swap(__float_as_uint(m), __float_as_uint(m), false, false);
    return fmaxf(__uint_as_float(rr[0]), __uint_as_float(rr[1]));
}
DI void exp_pack(const f32x16& s, bf16x8& p0, bf16x8& p1) {
    u32x4 a, b;
#pragma unroll
    for (int i = 0; i < 4; ++i) a[i] = cvtpk(__builtin_amdgcn_exp2f(s[2 * i]), __builtin_amdgcn_exp2f(s[2 * i + 1]));
#pragma unroll
    for (int i = 0; i < 4; ++i) b[i] = cvtpk(__builtin_amdgcn_exp2f(s[8 + 2 * i]), __builtin_amdgcn_exp2f(s[8 + 2 * i + 1]));
    p0 = __builtin_bit_cast(bf16x8, a); p1 = __builtin_bit_cast(bf16x8, b);
}

__global__ __launch_bounds__(512) void k2_attn(char* __restrict__ ws) {
    __shared__ __attribute__((aligned(16))) char lds[K2_LDS];
    const int tid = threadIdx.x, lane = tid & 63, w = __builtin_amdgcn_readfirstlane(tid >> 6);
    const int b = blockIdx.x, xcd = b & 7, loc = b >> 3, head = xcd * 4 + (loc >> 3), chunk = loc & 7;
    const int r = lane & 31, h = lane >> 5;
    const unsigned lds0 = lds_addr(lds);
    STAMP(1, 0);
    const char* kb = ws + WS_K + (size_t)head * 65536 + w * 1024;
    const char* vb = ws + WS_V + (size_t)head * 65536 + w * 1024;
    const unsigned vo = lane * 16;
#pragma unroll
    for (int c = 0; c < 8; ++c) { glds16(vo, kb + c * 8192, lds0 + K2_K + c * 8192 + w * 1024); glds16(vo, vb + c * 8192, lds0 + K2_V + c * 8192 + w * 1024); }
    const bf16x8 qf = *(const bf16x8*)(ws + WS_Q + (size_t)head * 65536 + (8 * chunk + w) * 1024 + lane * 16);
    { u32x4 ones = {0x3F803F80u, 0x3F803F80u, 0x3F803F80u, 0x3F803F80u}; if (tid < 320) *(LDSP u32x4*)((lds_ptr)lds + K2_ONES + tid * 16) = ones; }
    lds_cptr L = (lds_cptr)lds;
    lds_cptr kp = L + K2_K + lane * 16;
    const bool isv = r < 16;
    lds_cptr vp = isv ? L + K2_V + h * 256 + r * 16 : L + K2_ONES + ((r >= 20 && r < 28) ? 64 : 0);
    const unsigned vinc = isv ? 4096u : 0u;
    WAIT_BAR(14);
    STAMP(1, 1);
    f32x16 zero = {};
    bf16x8 kf = lds_rd128(kp);
    f32x16 s0 = MFMA32(kf, qf, zero);
    const float mref = max16(s0);
    f32x16 cneg;
#pragma unroll
    for (int i = 0; i < 16; ++i) { cneg[i] = -mref; s0[i] -= mref; }
    f32x16 o = {}, s1;
    kf = lds_rd128(kp + 1024);
    bf16x8 v0 = lds_rd128(vp), v1 = lds_rd128(vp + 512);
    u32x4 pa, pb = {0u, 0u, 0u, 0u};
#define EX(S, i) S[i] = __builtin_amdgcn_exp2f(S[i]); SBAR();
#define CV(P, S, i) P[i & 3] = cvtpk(S[2 * (i)], S[2 * (i) + 1]); SBAR();
#define STEP(SC, SN, u) \
        SN = MFMA32(kf, qf, cneg); SBAR(); \
        kf = lds_rd128(kq + ((u) + 2) * 1024); SBAR(); \
        EX(SC, 0) EX(SC, 1) EX(SC, 2) EX(SC, 3) \
        o = MFMA32(v1, __builtin_bit_cast(bf16x8, pb), o); SBAR(); \
        v1 = lds_rd128(vp + (u) * 1024 + 512); SBAR(); \
        CV(pa, SC, 0) EX(SC, 4) EX(SC, 5) CV(pa, SC, 1) EX(SC, 6) EX(SC, 7) CV(pa, SC, 2) EX(SC, 8) EX(SC, 9) CV(pa, SC, 3) EX(SC, 10) EX(SC, 11) \
        o = MFMA32(v0, __builtin_bit_cast(bf16x8, pa), o); SBAR(); \
        v0 = lds_rd128(vp + ((u) + 1) * 1024); SBAR(); \
        CV(pb, SC, 4) EX(SC, 12) EX(SC, 13) CV(pb, SC, 5) EX(SC, 14) EX(SC, 15) CV(pb, SC, 6) CV(pb, SC, 7)
    for (int it = 0; it < 16; ++it) {
        if (it == 1) WAIT_BAR(12);
        if (it == 3) WAIT_BAR(0);
        lds_cptr kq = kp + it * 4096;
        SBAR();
        STEP(s0, s1, 0) STEP(s1, s0, 1) STEP(s0, s1, 2) STEP(s1, s0, 3)
        vp += vinc;
    }
#undef STEP
#undef EX
#undef CV
    o = MFMA32(v1, __builtin_bit_cast(bf16x8, pb), o);
    bf16x8 p0, p1;
    STAMP(1, 2);
    float l = o[8];
    float chk = 0.f;
#pragma unroll
    for (int i = 0; i < 8; ++i) chk += fabsf(o[i]);
    const bool bad = !(l < 1.152921504606847e18f) || !(chk < 3.0e38f);
    if (__any(bad)) {
        float m = -INFINITY;
        o = f32x16{};
        lds_cptr vq = isv ? L + K2_V + h * 256 + r * 16 : L + K2_ONES;
        for (int t = 0; t < 64; ++t) {
            f32x16 s = MFMA32(lds_rd128(kp + t * 1024), qf, zero);
            const float mn = fmaxf(m, max16(s));
            const float f = __builtin_amdgcn_exp2f(m - mn);
            m = mn;
#pragma unroll
            for (int i = 0; i < 16; ++i) { s[i] -= mn; o[i] *= f; }
            exp_pack(s, p0, p1);
            lds_cptr vt = vq + (isv ? t * 1024 : 0);
            o = MFMA32(lds_rd128(vt), p0, o); o = MFMA32(lds_rd128(vt + 512), p1, o);
        }
        l = o[8];
    }
    const float inv = 1.f / l;
    u32x4 ov;
#pragma unroll
    for (int i = 0; i < 4; ++i) ov[i] = cvtpk(o[2 * i] * inv, o[2 * i + 1] * inv);
    *(u32x4*)(ws + WS_O + (size_t)((8 * chunk + w) * 32 + head) * 1024 + lane * 16) = ov;
    STAMP(1, 3);
}

DI unsigned top_key(const unsigned* __restrict__ keys) {
    unsigned m = 0;
#pragma unroll
    for (int i = 0; i < 64; ++i) { const unsigned k = keys[i]; m = k > m ? k : m; }
    return m;
}
__global__ __launch_bounds__(256) void k_ffn(const u16* __restrict__ x, const u16* __restrict__ wg, const u16* __restrict__ wu, const u16* __restrict__ wd, char* __restrict__ ws) {
    __shared__ float red[2][16][16];
    __shared__ float hs[16];
    const int t = threadIdx.x, u = blockIdx.x;
    const unsigned key = top_key((const unsigned*)(ws + WS_KEYS));
    const int idx = 0xFFFF - (key & 0xFFFF);
    const int c = t & 15, kg = t >> 4;
    float a = 0.f, bsum = 0.f;
    for (int k = kg * 32; k < kg * 32 + 32; ++k) { const float xv = bf2f(x[idx * DM + k]); a += xv * bf2f(wg[k * FF + 16 * u + c]); bsum += xv * bf2f(wu[k * FF + 16 * u + c]); }
    red[0][kg][c] = a; red[1][kg][c] = bsum;
    __syncthreads();
    if (t < 16) {
        float g = 0.f, uu = 0.f;
        for (int i = 0; i < 16; ++i) { g += red[0][i][t]; uu += red[1][i][t]; }
        g = rbf(g); uu = rbf(uu);
        const float sg = rbf(g / (1.f + __expf(-g)));
        hs[t] = rbf(sg * uu);
    }
    __syncthreads();
    for (int n = t; n < DM; n += 256) {
        float y = 0.f;
        for (int f = 0; f < 16; ++f) y += hs[f] * bf2f(wd[(16 * u + f) * DM + n]);
        ((float*)(ws + WS_YP))[u * DM + n] = y;
    }
}

constexpr int K3_Y = 8 * 16384, K3_LDS = K3_Y + 1024;

__global__ __launch_bounds__(256) void k3_out(const u16* __restrict__ x, const u16* __restrict__ wo, char* __restrict__ ws, u16* __restrict__ out) {
    __shared__ __attribute__((aligned(16))) char lds[K3_LDS];
    const int tid = threadIdx.x, lane = tid & 63, w = __builtin_amdgcn_readfirstlane(tid >> 6);
    const int b = blockIdx.x, xcd = b & 7, loc = b >> 3, tp = xcd * 4 + (loc & 3), ct = loc >> 2;
    const int r = lane & 31, h = lane >> 5;
    const unsigned lds0 = lds_addr(lds);
    STAMP(2, 0);
    const char* ob = ws + WS_O + (size_t)(2 * tp + (w >> 1)) * 32768 + (w & 1) * 2048;
    const char* wb = (const char*)wo + (size_t)((w & 1) * 32) * 1024 + ct * 128 + (w >> 1) * 64;
    const unsigned ovo = lane * 16, wvo = (lane >> 2) * 1024 + (lane & 3) * 16;
#pragma unroll
    for (int s = 0; s < 8; ++s) {
        const unsigned sl = lds0 + s * 16384;
#pragma unroll
        for (int q = 0; q < 2; ++q) glds16(ovo, ob + s * 4096 + q * 1024, sl + (2 * w + q) * 1024);
#pragma unroll
        for (int q = 0; q < 2; ++q) glds16(wvo, wb + (size_t)(64 * s + 16 * q) * 1024, sl + 8192 + (2 * w + q) * 1024);
    }
    const int tt = w & 1, g = w >> 1;
    const int token = 64 * tp + 32 * tt + r, col = 64 * ct + 32 * g + 8 * h;
    const u32x4 x0 = *(const u32x4*)(x + token * DM + col), x1 = *(const u32x4*)(x + token * DM + col + 16);
    const float gt = ((const float*)(ws + WS_GATE))[token];
    const unsigned key = top_key((const unsigned*)(ws + WS_KEYS));
    const int idx = 0xFFFF - (key & 0xFFFF);
    const bool owner = (idx >> 6) == tp;
    LDSP float* yl = (LDSP float*)((lds_ptr)lds + K3_Y);
    if (owner) {
        const float* yp = (const float*)(ws + WS_YP) + 64 * ct + lane;
        float y = 0.f;
        for (int u = w; u < 86; u += 4) y += yp[u * DM];
        yl[w * 64 + lane] = y;
    }
    const int blk = (lane >> 4) & 1, q4 = (lane & 15) >> 2, p4 = lane & 3;
    const unsigned wq = 8192 + g * 4096 + (8 * h + q4) * 64 + blk * 32 + (((p4 & 1) << 1) | (p4 >> 1)) * 8;
    const unsigned oo = tt * 4096 + lane * 16;
    lds_cptr L = (lds_cptr)lds;
    f32x16 acc = {};
#define K3_STEP(s, N) WAIT_BAR(N); { _Pragma("unroll") for (int ks = 0; ks < 4; ++ks) { bf16x8 of = lds_rd128(L + (s) * 16384 + oo + ks * 1024); bf16x8 wf = lds_tr2(L + (s) * 16384 + wq + ks * 1024); acc = MFMA32(wf, of, acc); } } SBAR();
    K3_STEP(0, 28) STAMP(2, 1); K3_STEP(1, 24) K3_STEP(2, 20) K3_STEP(3, 16) K3_STEP(4, 12) K3_STEP(5, 8) K3_STEP(6, 4) K3_STEP(7, 0)
    STAMP(2, 2);
#undef K3_STEP
    const float omg = rbf(1.f - gt);
    float ysel[16];
#pragma unroll
    for (int i = 0; i < 16; ++i) ysel[i] = 0.f;
    const bool sel = token == idx;
    if (owner) {
        const unsigned ob16 = key >> 16, rb = (ob16 & 0x8000u) ? (ob16 & 0x7FFFu) : (~ob16 & 0xFFFFu);
        const float rs = rbf(1.f / (1.f + __expf(-bf2f(rb))));
#pragma unroll
        for (int i = 0; i < 16; ++i) { const int c = 32 * g + 8 * h + (i & 7) + 16 * (i >> 3); const float y = rbf((yl[c] + yl[64 + c]) + (yl[128 + c] + yl[192 + c])); ysel[i] = sel ? rbf(y * rs) : 0.f; }
    }
#pragma unroll
    for (int e = 0; e < 2; ++e) {
        const u32x4 xv = e ? x1 : x0;
        u32x4 ov;
#pragma unroll
        for (int i = 0; i < 4; ++i) {
            float o2[2];
#pragma unroll
            for (int j = 0; j < 2; ++j) {
                const float xx = j ? __uint_as_float(xv[i] & 0xFFFF0000u) : __uint_as_float(xv[i] << 16);
                const float bb = rbf(xx + ysel[8 * e + 2 * i + j]);
                const float a = rbf(acc[8 * e + 2 * i + j]);
                o2[j] = rbf(gt * a) + rbf(omg * bb);
            }
            ov[i] = cvtpk(o2[0], o2[1]);
        }
        *(u32x4*)(out + token * DM + col + 16 * e) = ov;
    }
    STAMP(2, 3);
}

extern "C" void kernel_launch(void* const* d_in, const int* in_sizes, int n_in, void* d_out, int out_size, void* d_ws, size_t ws_size, hipStream_t stream) {
    const u16* x = (const u16*)d_in[0];
    const u16* wqkv = (const u16*)d_in[1];
    const u16* wo = (const u16*)d_in[2];
    const u16* w_r = (const u16*)d_in[4];
    const u16* wg = (const u16*)d_in[5];
    const u16* wu = (const u16*)d_in[6];
    const u16* wd = (const u16*)d_in[7];
    const u16* gate_w = (const u16*)d_in[8];
    char* ws = (char*)d_ws;
    k1_qkv<<<256, 256, 0, stream>>>(x, wqkv, w_r, gate_w, ws);
    k_ffn<<<86, 256, 0, stream>>>(x, wg, wu, wd, ws);
    k2_attn<<<256, 512, 0, stream>>>(ws);
    k3_out<<<256, 256, 0, stream>>>(x, wo, ws, (u16*)d_out);
}
```

```cpp
#include <hip/hip_runtime.h>
#include <hip/hip_bf16.h>
#include <stdint.h>

typedef __bf16 bf16x8 __attribute__((ext_vector_type(8)));
typedef __bf16 bf16x2 __attribute__((ext_vector_type(2)));
typedef short s16x4 __attribute__((ext_vector_type(4)));
typedef short s16x8 __attribute__((ext_vector_type(8)));
typedef float f32x16 __attribute__((ext_vector_type(16)));
typedef float f32x4 __attribute__((ext_vector_type(4)));
typedef float f32x2 __attribute__((ext_vector_type(2)));
typedef unsigned u32x4 __attribute__((ext_vector_type(4)));
typedef unsigned short u16;
#define LDSP __attribute__((address_space(3)))
typedef LDSP const char* lds_cptr;
typedef LDSP char* lds_ptr;
#define DI __device__ __forceinline__
#define MFMA32(a, b, c) __builtin_amdgcn_mfma_f32_32x32x16_bf16(a, b, c, 0, 0, 0)
#define SBAR() __builtin_amdgcn_sched_barrier(0)

#define MAPPING 0
#define STAMP(kid, i)
constexpr int SEQ = 2048, DM = 512, NH = 32, FF = 1376;
constexpr float QSCALE = 0.25f * 1.4426950408889634f;

constexpr size_t WS_Q = 0, WS_K = 2u << 20, WS_V = 4u << 20, WS_O = 6u << 20;
constexpr size_t WS_GATE = 8u << 20;
constexpr size_t WS_KEYS = WS_GATE + 8192;
constexpr size_t WS_H = WS_KEYS + 1024;
constexpr size_t WS_YP = WS_H + 8192;
constexpr size_t WS_EPOCH = 9u << 20;
constexpr size_t WS_FLAG_A = WS_EPOCH + 4096;
constexpr size_t WS_FLAG_F = WS_FLAG_A + 32768;
constexpr size_t WS_Y2 = WS_FLAG_F + 16384;

DI unsigned cvtpk(float lo, float hi) { f32x2 v = {lo, hi}; bf16x2 b = __builtin_convertvector(v, bf16x2); return __builtin_bit_cast(unsigned, b); }
DI float bf2f(unsigned b) { return __uint_as_float(b << 16); }
DI float rbf(float x) { return __uint_as_float(cvtpk(x, 0.f) << 16); }
DI unsigned lds_addr(const void* p) { return (unsigned)(size_t)(LDSP const char*)p; }
DI void glds16(unsigned voff, const void* sbase, unsigned ldsdst) {
    asm volatile("s_mov_b32 m0, %2\n\ts_nop 0\n\tglobal_load_lds_dwordx4 %0, %1" ::"v"(voff), "s"(sbase), "s"(ldsdst) : "memory");
}
DI bf16x8 lds_rd128(lds_cptr p) { return *(LDSP const bf16x8*)p; }
DI s16x4 lds_tr(lds_cptr p) { return __builtin_bit_cast(s16x4, __builtin_amdgcn_ds_read_tr16_b64_v4i16((LDSP s16x4*)p)); }
DI bf16x8 lds_tr2(lds_cptr p) {
    s16x4 a = lds_tr(p), b = lds_tr(p + 256);
    s16x8 r = {a[0], a[1], a[2], a[3], b[0], b[1], b[2], b[3]};
    return __builtin_bit_cast(bf16x8, r);
}
#define WAIT_BAR(N) asm volatile("s_waitcnt vmcnt(" #N ") lgkmcnt(0)\n\ts_barrier" ::: "memory")
DI int swap23(int v) { return (v & ~12) | ((v & 4) << 1) | ((v & 8) >> 1); }

constexpr int AB_SLOT = 39168, AB_W = 32768, AB_AUX = 4 * AB_SLOT;
constexpr int AB_K = 0, AB_V = 65536, AB_ONES = 131072;
constexpr int AB_FS = AB_ONES + 5632, AB_FH = AB_FS + 1024, AB_FC = AB_FH + 64;
constexpr int AB_LAND = AB_FC + 64;
constexpr int AB_FW = AB_FS + 1280;
constexpr int AB_LDS = AB_AUX + 4096;
static_assert(AB_LDS <= 163840 && AB_FW + 16384 <= AB_LDS && AB_FW % 256 == 0, "LDS");

DI float max16(const f32x16& s) {
    float a = fmaxf(fmaxf(s[0], s[1]), s[2]), b = fmaxf(fmaxf(s[3], s[4]), s[5]);
    a = fmaxf(fmaxf(a, s[6]), s[7]); b = fmaxf(fmaxf(b, s[8]), s[9]);
    a = fmaxf(fmaxf(a, s[10]), s[11]); b = fmaxf(fmaxf(b, s[12]), s[13]);
    a = fmaxf(fmaxf(a, s[14]), s[15]);
    float m = fmaxf(a, b);
    auto rr = __builtin_amdgcn_permlane32_swap(__float_as_uint(m), __float_as_uint(m), false, false);
    return fmaxf(__uint_as_float(rr[0]), __uint_as_float(rr[1]));
}
DI void exp_pack(const f32x16& s, bf16x8& p0, bf16x8& p1) {
    u32x4 a, b;
#pragma unroll
    for (int i = 0; i < 4; ++i) a[i] = cvtpk(__builtin_amdgcn_exp2f(s[2 * i]), __builtin_amdgcn_exp2f(s[2 * i + 1]));
#pragma unroll
    for (int i = 0; i < 4; ++i) b[i] = cvtpk(__builtin_amdgcn_exp2f(s[8 + 2 * i]), __builtin_amdgcn_exp2f(s[8 + 2 * i + 1]));
    p0 = __builtin_bit_cast(bf16x8, a); p1 = __builtin_bit_cast(bf16x8, b);
}
DI void st16_dev(unsigned voff, u32x4 d, void* sbase) { asm volatile("global_store_dwordx4 %0, %1, %2 sc1\n\ts_nop 1" ::"v"(voff), "v"(d), "s"(sbase) : "memory"); }
DI void glds16_dev(unsigned voff, const void* sbase, unsigned ldsdst) {
    asm volatile("s_mov_b32 m0, %2\n\ts_nop 0\n\tglobal_load_lds_dwordx4 %0, %1 sc1" ::"v"(voff), "s"(sbase), "s"(ldsdst) : "memory");
}
typedef unsigned long long u64;
DI u64 ld_dev(const void* p) { return __hip_atomic_load((const u64*)p, __ATOMIC_RELAXED, __HIP_MEMORY_SCOPE_AGENT); }
DI void st_dev(void* p, u64 v) { __hip_atomic_store((u64*)p, v, __ATOMIC_RELAXED, __HIP_MEMORY_SCOPE_AGENT); }
DI void wait_flags(const char* flag, bool want, u64 tag) {
#pragma unroll 1
    for (unsigned spin = 0; spin < (1u << 20); ++spin) {
        const u64 v = want ? ld_dev(flag) : tag;
        if (__all(v == tag)) break;
        __builtin_amdgcn_s_sleep(1);
    }
}

#define BARRIER() asm volatile("s_barrier" ::: "memory")
#define WAIT_V(N) asm volatile("s_waitcnt vmcnt(" #N ")" ::: "memory")

DI f32x4 mfma16(bf16x8 a, bf16x8 b, f32x4 c) { return __builtin_amdgcn_mfma_f32_16x16x32_bf16(a, b, c, 0, 0, 0); }
DI void lds_count(LDSP unsigned* c) { asm volatile("s_waitcnt vmcnt(0) lgkmcnt(0)" ::: "memory"); if ((threadIdx.x & 63) == 0) __hip_atomic_fetch_add(c, 1u, __ATOMIC_RELAXED, __HIP_MEMORY_SCOPE_WORKGROUP); }
DI void lds_wait(LDSP unsigned* c, unsigned n) {
#pragma unroll 1
    for (unsigned spin = 0; spin < (1u << 20); ++spin) {
        if (__hip_atomic_load(c, __ATOMIC_RELAXED, __HIP_MEMORY_SCOPE_WORKGROUP) >= n) break;
        __builtin_amdgcn_s_sleep(1);
    }
    asm volatile("" ::: "memory");
}
DI unsigned top_key_dev(char* ws, int lane, u64 tag) {
    wait_flags(ws + WS_FLAG_A + lane * 128, true, tag);
    unsigned key = __hip_atomic_load((const unsigned*)(ws + WS_KEYS) + lane, __ATOMIC_RELAXED, __HIP_MEMORY_SCOPE_AGENT);
#pragma unroll
    for (int o = 32; o >= 1; o >>= 1) { const unsigned t = __shfl_xor(key, o); key = t > key ? t : key; }
    return __builtin_amdgcn_readfirstlane(key);
}
DI bf16x8 tr_frag(lds_cptr p, int step) { s16x4 a = lds_tr(p), c = lds_tr(p + step); s16x8 t = {a[0], a[1], a[2], a[3], c[0], c[1], c[2], c[3]}; return __builtin_bit_cast(bf16x8, t); }
DI void ffn_unit(char* lds, const u16* x, const u16* wg, const u16* wu, const u16* wd, char* ws, int u, int j, int lane, u64 tag) {
    const int n = lane & 15, g = lane >> 4, q = n >> 2, p = n & 3;
    const unsigned slot = lds_addr(lds) + AB_FW + j * 4096;
    lds_cptr sl = (lds_cptr)lds + AB_FW + j * 4096;
    bf16x8 ag[4], au[4], ad[8];
    const unsigned vo = (lane >> 1) * (FF * 2) + (lane & 1) * 16;
    const size_t wo = (size_t)(128 * j) * (FF * 2) + 32 * u;
#pragma unroll
    for (int i = 0; i < 4; ++i) glds16(vo, (const char*)wg + wo + (size_t)i * 32 * (FF * 2), slot + i * 1024);
    asm volatile("s_waitcnt vmcnt(0)" ::: "memory");
#pragma unroll
    for (int ks = 0; ks < 4; ++ks) ag[ks] = tr_frag(sl + (32 * ks + 8 * g + q) * 32 + p * 8, 128);
    asm volatile("s_waitcnt lgkmcnt(0)" : "+v"(ag[0]), "+v"(ag[1]), "+v"(ag[2]), "+v"(ag[3])::"memory");
#pragma unroll
    for (int i = 0; i < 4; ++i) glds16(vo, (const char*)wu + wo + (size_t)i * 32 * (FF * 2), slot + i * 1024);
    asm volatile("s_waitcnt vmcnt(0)" ::: "memory");
#pragma unroll
    for (int ks = 0; ks < 4; ++ks) au[ks] = tr_frag(sl + (32 * ks + 8 * g + q) * 32 + p * 8, 128);
    asm volatile("s_waitcnt lgkmcnt(0)" : "+v"(au[0]), "+v"(au[1]), "+v"(au[2]), "+v"(au[3])::"memory");
#pragma unroll
    for (int i = 0; i < 4; ++i) glds16((lane >> 4) * 1024 + (lane & 15) * 16, (const char*)wd + (size_t)(16 * u + 4 * i) * 1024 + j * 256, slot + i * 1024);
    const unsigned key = top_key_dev(ws, lane, tag);
    const int idx = 0xFFFF - (key & 0xFFFF);
    bf16x8 xs[4];
#pragma unroll
    for (int ks = 0; ks < 4; ++ks) xs[ks] = *(const bf16x8*)(x + idx * DM + 128 * j + 32 * ks + 8 * g);
    asm volatile("s_waitcnt vmcnt(0)" ::: "memory");
#pragma unroll
    for (int t = 0; t < 8; ++t) ad[t] = tr_frag(sl + (8 * (g & 1) + q) * 256 + t * 32 + p * 8, 1024);
    f32x4 dg = {}, du = {};
#pragma unroll
    for (int ks = 0; ks < 4; ++ks) { dg = mfma16(ag[ks], xs[ks], dg); du = mfma16(au[ks], xs[ks], du); }
    LDSP float* fs = (LDSP float*)((lds_ptr)lds + AB_FS);
    LDSP unsigned* fc = (LDSP unsigned*)((lds_ptr)lds + AB_FC);
    if (n == 0) { *(LDSP f32x4*)(fs + j * 32 + 4 * g) = dg; *(LDSP f32x4*)(fs + j * 32 + 16 + 4 * g) = du; }
    lds_count(fc);
    if (j == 0) {
        lds_wait(fc, 4);
        const int l32 = lane & 31;
        const float v = rbf((fs[l32] + fs[32 + l32]) + (fs[64 + l32] + fs[96 + l32]));
        const float up = __shfl(v, (lane & 15) + 16);
        const float hv = rbf(v / (1.f + __expf(-v))) * up;
        if (lane < 16) *(LDSP u16*)((lds_ptr)lds + AB_FH + lane * 2) = (u16)(cvtpk(hv, 0.f) & 0xFFFFu);
        lds_count(fc + 1);
    }
    lds_wait(fc + 1, 1);
    u32x4 hb = *(LDSP const u32x4*)((lds_ptr)lds + AB_FH + (g & 1) * 16);
    if (g >= 2) hb = u32x4{0u, 0u, 0u, 0u};
    char* yb = ws + WS_YP + (size_t)u * 2048 + j * 512;
    f32x4 y[8];
#pragma unroll
    for (int t = 0; t < 8; ++t) y[t] = mfma16(ad[t], __builtin_bit_cast(bf16x8, hb), f32x4{});
    asm volatile("s_nop 7\n\ts_nop 1" : "+v"(y[0]), "+v"(y[1]), "+v"(y[2]), "+v"(y[3]), "+v"(y[4]), "+v"(y[5]), "+v"(y[6]), "+v"(y[7]));
    if (n == 0) {
#pragma unroll
        for (int t = 0; t < 8; ++t) st16_dev(t * 64 + g * 16, __builtin_bit_cast(u32x4, y[t]), yb);
    }
    lds_count(fc + 2);
    if (j == 0) { lds_wait(fc + 2, 4); if (lane == 0) st_dev(ws + WS_FLAG_F + u * 128, tag); }
}
DI void ffn_reduce(char* lds, char* ws, int g8, int j, int lane, u64 tag) {
    const unsigned key = top_key_dev(ws, lane, tag);
    wait_flags(ws + WS_FLAG_F + lane * 128, true, tag);
    wait_flags(ws + WS_FLAG_F + (64 + lane) * 128, lane < 22, tag);
    const unsigned* yp = (const unsigned*)(ws + WS_YP) + 64 * g8 + lane;
    unsigned v[22];
#pragma unroll
    for (int i = 0; i < 22; ++i) { const int u = min(j + 4 * i, 85); v[i] = __hip_atomic_load(yp + u * DM, __ATOMIC_RELAXED, __HIP_MEMORY_SCOPE_AGENT); }
    float y = 0.f;
#pragma unroll
    for (int i = 0; i < 22; ++i) y += (j + 4 * i < 86) ? __uint_as_float(v[i]) : 0.f;
    LDSP float* fs = (LDSP float*)((lds_ptr)lds + AB_FS);
    LDSP unsigned* fc = (LDSP unsigned*)((lds_ptr)lds + AB_FC);
    fs[j * 64 + lane] = y;
    lds_count(fc);
    if (j == 0) {
        lds_wait(fc, 4);
        const float ys = rbf((fs[lane] + fs[64 + lane]) + (fs[128 + lane] + fs[192 + lane]));
        const unsigned ob16 = key >> 16, rb = (ob16 & 0x8000u) ? (ob16 & 0x7FFFu) : (~ob16 & 0xFFFFu);
        const float rs = rbf(1.f / (1.f + __expf(-bf2f(rb))));
        __hip_atomic_store((unsigned*)(ws + WS_Y2) + 64 * g8 + lane, __float_as_uint(rbf(ys * rs)), __ATOMIC_RELAXED, __HIP_MEMORY_SCOPE_AGENT);
    }
}

DI void ab_loader(char* lds, const u16* x, const u16* wqkv, const u16* w_r, const u16* gate_w, const u16* wg, const u16* wu, const u16* wd, char* ws, int head, int chunk, int j, int lane) {
    const unsigned lds0 = lds_addr(lds);
    const int rot = (head + chunk) & 7, t = 64 * j + lane;
    __builtin_amdgcn_s_setprio(3);
    unsigned e_lo, e_hi, a0, a1, a2, a3;
    asm volatile("global_load_dword %0, %6, %7 sc1\n\tglobal_load_dword %1, %6, %7 offset:4 sc1\n\t"
                 "global_load_ushort %2, %8, %9\n\tglobal_load_ushort %3, %8, %10\n\tglobal_load_ushort %4, %8, %9 offset:512\n\tglobal_load_ushort %5, %8, %10 offset:512"
                 : "=&v"(e_lo), "=&v"(e_hi), "=&v"(a0), "=&v"(a1), "=&v"(a2), "=&v"(a3) : "v"(0u), "s"(ws + WS_EPOCH), "v"(t * 2), "s"(w_r), "s"(gate_w) : "memory");
    const unsigned xv0 = (lane >> 3) * 1024 + ((lane & 7) ^ ((lane >> 4) & 7)) * 16;
    const unsigned xv1 = (lane >> 3) * 1024 + ((lane & 7) ^ (((lane >> 4) + 4) & 7)) * 16;
    unsigned wsrc[2];
#pragma unroll
    for (int e = 0; e < 2; ++e) {
        const int o = 768 * (2 * j + e) + 16 * lane;
        if (o < 4096) { const int cc = (o >> 4) & 3; wsrc[e] = (o >> 6) * 3072 + (cc < 2 ? 16 * cc : 1024 + 16 * (cc - 2)); }
        else { const int o2 = o - 4096, row = min(o2 >> 5, 63); wsrc[e] = row * 3072 + 2048 + 16 * ((o2 >> 4) & 1); }
    }
    const char* xb = (const char*)x + (size_t)(256 * chunk + 64 * j) * 1024;
    const char* wb = (const char*)wqkv + head * 32;
    auto issue = [&](int i) {
        const int s = (rot + i) & 7;
        const unsigned sl = lds0 + (i & 3) * AB_SLOT;
        if (lane < 48) { glds16(wsrc[0], wb + (size_t)s * 64 * 3072, sl + AB_W + 768 * (2 * j)); glds16(wsrc[1], wb + (size_t)s * 64 * 3072, sl + AB_W + 768 * (2 * j + 1)); }
#pragma unroll
        for (int q = 0; q < 8; ++q) glds16((q & 1) ? xv1 : xv0, xb + q * 8192 + s * 128, sl + (8 * j + q) * 1024);
    };
    STAMP(3, 1); issue(0); STAMP(3, 2); issue(1); STAMP(3, 3);
    asm volatile("s_waitcnt vmcnt(10)" : "+v"(e_lo), "+v"(e_hi), "+v"(a0), "+v"(a1), "+v"(a2), "+v"(a3)::"memory");
    *(LDSP unsigned*)((lds_ptr)lds + AB_AUX + t * 8) = a0 | (a1 << 16);
    *(LDSP unsigned*)((lds_ptr)lds + AB_AUX + (t + 256) * 8) = a2 | (a3 << 16);
    const u64 tag = (((u64)__builtin_amdgcn_readfirstlane(e_hi) << 32) | __builtin_amdgcn_readfirstlane(e_lo)) + 1;
    STAMP(3, 4);
    WAIT_BAR(10); issue(2); STAMP(3, 5);
    WAIT_BAR(10); STAMP(3, 6); issue(3); STAMP(3, 7);
    WAIT_BAR(10); issue(4);
    WAIT_BAR(10); issue(5);
    WAIT_BAR(10); issue(6);
    WAIT_BAR(10); issue(7);
    WAIT_BAR(10);
    WAIT_BAR(0);
    BARRIER();
    if (j == 0 && lane < 8) *(LDSP unsigned*)((lds_ptr)lds + AB_FC + (lane & 3) * 4 + (lane >> 2) * 64) = 0u;
    asm volatile("s_waitcnt lgkmcnt(0)\n\ts_barrier" ::: "memory");
    if (j == 0 && lane == 0) st_dev(ws + WS_FLAG_A + (head * 8 + chunk) * 128, tag);
    wait_flags(ws + WS_FLAG_A + (head * 8 + (lane & 7)) * 128, lane < 8, tag);
    STAMP(2, 0);
    {
        const char* kb = ws + WS_K + (size_t)head * 65536 + j * 2048;
        const char* vb = ws + WS_V + (size_t)head * 65536 + j * 2048;
        auto fetch = [&](int p) {
            const int cp = (chunk + p) & 7;
            glds16_dev(lane * 16, kb + cp * 8192, lds0 + AB_K + p * 8192 + j * 2048); glds16_dev(lane * 16, kb + cp * 8192 + 1024, lds0 + AB_K + p * 8192 + j * 2048 + 1024);
            glds16_dev(lane * 16, vb + cp * 8192, lds0 + AB_V + p * 8192 + j * 2048); glds16_dev(lane * 16, vb + cp * 8192 + 1024, lds0 + AB_V + p * 8192 + j * 2048 + 1024);
        };
        LDSP volatile unsigned* land = (LDSP volatile unsigned*)((lds_ptr)lds + AB_LAND) + j;
#define LANDED(N, p) do { WAIT_V(N); if (lane == 0) *land = p; } while (0)
        fetch(1); fetch(2);
        LANDED(4, 1);
        STAMP(2, 1);
        fetch(3); fetch(4); fetch(5); fetch(6); fetch(7);
        LANDED(20, 2); LANDED(16, 3); LANDED(12, 4); LANDED(8, 5); LANDED(4, 6); LANDED(0, 7);
#undef LANDED
        STAMP(2, 2);
    }
    if (blockIdx.x < 86) ffn_unit(lds, x, wg, wu, wd, ws, blockIdx.x, j, lane, tag);
    else if (blockIdx.x < 94) ffn_reduce(lds, ws, blockIdx.x - 86, j, lane, tag);
    STAMP(2, 3);
    if (blockIdx.x == 255 && j == 0) {
#pragma unroll
        for (int i = 0; i < 4; ++i) wait_flags(ws + WS_FLAG_A + (64 * i + lane) * 128, true, tag);
        if (lane == 0) st_dev(ws + WS_EPOCH, tag);
    }
}

__global__ __launch_bounds__(768) void kab(const u16* __restrict__ x, const u16* __restrict__ wqkv, const u16* __restrict__ w_r, const u16* __restrict__ gate_w,
                                          const u16* __restrict__ wg, const u16* __restrict__ wu, const u16* __restrict__ wd, char* __restrict__ ws) {
    __shared__ __attribute__((aligned(16))) char lds[AB_LDS];
    const int tid = threadIdx.x, lane = tid & 63, w = __builtin_amdgcn_readfirstlane(tid >> 6);
    const int b = blockIdx.x, xcd = b & 7, loc = b >> 3;
    const int head = xcd * 4 + (loc >> 3), chunk = loc & 7;
    if (w >= 8) { ab_loader(lds, x, wqkv, w_r, gate_w, wg, wu, wd, ws, head, chunk, w - 8, lane); return; }
    const int r = lane & 31, h = lane >> 5;
    lds_cptr L = (lds_cptr)lds;
    const int rot = (head + chunk) & 7;
    unsigned xo[4];
#pragma unroll
    for (int ks = 0; ks < 4; ++ks) xo[ks] = (32 * w + r) * 128 + ((2 * ks + h) ^ ((r >> 1) & 7)) * 16;
    const int blk = (lane >> 4) & 1, q4 = (lane & 15) >> 2, p4 = lane & 3;
    const unsigned wq = AB_W + (8 * h + q4) * 64 + blk * 32 + (((p4 & 1) << 1) | (p4 >> 1)) * 8;
    const unsigned wva = blk ? (8 * h + q4) * 8 : (8 * h + q4) * 32 + p4 * 8, wvk = blk ? 128 : 512;
    f32x16 d1 = {}, d2 = {};
    bf16x8 xa[2], qa[2], va[2], xb2[2], qb[2], vb2[2];
    auto rd = [&](int i, int k0, bf16x8 (&xf)[2], bf16x8 (&fq)[2], bf16x8 (&fv)[2]) {
        const int s = (rot + i) & 7;
        lds_cptr sl = L + (i & 3) * AB_SLOT;
        lds_cptr vv = L + wva + (blk ? AB_AUX + s * 512 : (i & 3) * AB_SLOT + AB_W + 4096);
#pragma unroll
        for (int e = 0; e < 2; ++e) {
            const int ks = k0 + e;
            xf[e] = lds_rd128(sl + xo[ks]);
            fq[e] = lds_tr2(sl + wq + ks * 1024);
            s16x4 a = lds_tr(vv + ks * wvk), c = lds_tr(vv + ks * wvk + (wvk >> 2));
            s16x8 t = {a[0], a[1], a[2], a[3], c[0], c[1], c[2], c[3]};
            fv[e] = __builtin_bit_cast(bf16x8, t);
        }
        SBAR();
    };
    auto mm = [&](bf16x8 (&xf)[2], bf16x8 (&fq)[2], bf16x8 (&fv)[2]) {
#pragma unroll
        for (int e = 0; e < 2; ++e) { d1 = MFMA32(fq[e], xf[e], d1); d2 = MFMA32(xf[e], fv[e], d2); }
        SBAR();
    };
#define LBAR() asm volatile("s_waitcnt lgkmcnt(0)\n\ts_barrier" ::: "memory")
    LBAR(); STAMP(0, 0);
    rd(0, 0, xa, qa, va);
#define A_STAGE(i) rd(i, 2, xb2, qb, vb2); mm(xa, qa, va); if ((i) < 7) { LBAR(); rd((i) + 1, 0, xa, qa, va); } mm(xb2, qb, vb2);
    A_STAGE(0) A_STAGE(1) A_STAGE(2) A_STAGE(3) A_STAGE(4) A_STAGE(5) A_STAGE(6) A_STAGE(7)
#undef A_STAGE
    STAMP(0, 1);
    u32x4 qw, kw, vw0, vw1;
#pragma unroll
    for (int i = 0; i < 4; ++i) {
        kw[i] = cvtpk(d1[8 + 2 * i], d1[8 + 2 * i + 1]);
        vw0[i] = cvtpk(d2[2 * i], d2[2 * i + 1]); vw1[i] = cvtpk(d2[8 + 2 * i], d2[8 + 2 * i + 1]);
    }
    const bool isv = r < 16;
    const unsigned vpos = h * 256 + swap23(r & 15) * 16;
    {
        char* kg = ws + WS_K + (size_t)head * 65536 + (8 * chunk + w) * 1024;
        char* vg = ws + WS_V + (size_t)head * 65536 + (8 * chunk + w) * 1024;
        st16_dev(lane * 16, kw, kg);
        if (isv) { st16_dev(vpos, vw0, vg); st16_dev(vpos + 512, vw1, vg); }
    }
#pragma unroll
    for (int i = 0; i < 4; ++i) qw[i] = cvtpk(d1[2 * i] * QSCALE, d1[2 * i + 1] * QSCALE);
    const bf16x8 qf = __builtin_bit_cast(bf16x8, qw);
    if (w == head) {
        LDSP float* rg = (LDSP float*)((lds_ptr)lds + 2 * AB_SLOT + w * 4096);
        if ((r & 30) == 16) {
#pragma unroll
            for (int qd = 0; qd < 4; ++qd) *(LDSP f32x4*)(rg + (r & 1) * 32 + 8 * qd + 4 * h) = f32x4{d2[4 * qd], d2[4 * qd + 1], d2[4 * qd + 2], d2[4 * qd + 3]};
        }
        const int token = 256 * chunk + 32 * w + r;
        const unsigned rb = cvtpk(rg[r], 0.f) & 0xFFFFu;
        unsigned key = (((rb & 0x8000u) ? (~rb & 0xFFFFu) : (rb | 0x8000u)) << 16) | (0xFFFFu - token);
#pragma unroll
        for (int o = 16; o >= 1; o >>= 1) { const unsigned t = __shfl_xor(key, o); key = t > key ? t : key; }
        if (lane == 0) __hip_atomic_store((unsigned*)(ws + WS_KEYS) + 8 * chunk + w, key, __ATOMIC_RELAXED, __HIP_MEMORY_SCOPE_AGENT);
        const float gs = rbf(rg[32 + r]);
        const float g = rbf(1.f / (1.f + __expf(-gs)));
        if (lane < 32) __hip_atomic_store((unsigned*)(ws + WS_GATE) + token, __float_as_uint(g), __ATOMIC_RELAXED, __HIP_MEMORY_SCOPE_AGENT);
    }
    LBAR();
    *(LDSP u32x4*)((lds_ptr)lds + AB_K + w * 1024 + lane * 16) = kw;
    if (isv) { *(LDSP u32x4*)((lds_ptr)lds + AB_V + w * 1024 + vpos) = vw0; *(LDSP u32x4*)((lds_ptr)lds + AB_V + w * 1024 + vpos + 512) = vw1; }
    { u32x4 ones = {0x3F803F80u, 0x3F803F80u, 0x3F803F80u, 0x3F803F80u}; if (tid < 352) *(LDSP u32x4*)((lds_ptr)lds + AB_ONES + tid * 16) = ones; }
    WAIT_BAR(0);
    STAMP(0, 2);
    lds_cptr kp = L + AB_K + lane * 16;
    lds_cptr vp = isv ? L + AB_V + h * 256 + r * 16 : L + AB_ONES + ((r >= 20 && r < 28) ? 64 : 0);
    const unsigned vinc = isv ? 4096u : 0u;
    f32x16 zero = {};
    bf16x8 kf = lds_rd128(kp);
    f32x16 s0 = MFMA32(kf, qf, zero);
    const float mref = max16(s0);
    f32x16 cneg;
#pragma unroll
    for (int i = 0; i < 16; ++i) { cneg[i] = -mref; s0[i] -= mref; }
    f32x16 o = {}, s1;
    kf = lds_rd128(kp + 1024);
    bf16x8 v0 = lds_rd128(vp), v1 = lds_rd128(vp + 512);
    u32x4 pa, pb = {0u, 0u, 0u, 0u};
#define EX(S, i) S[i] = __builtin_amdgcn_exp2f(S[i]); SBAR();
#define CV(P, S, i) P[i & 3] = cvtpk(S[2 * (i)], S[2 * (i) + 1]); SBAR();
#define STEP(SC, SN, u) \
        SN = MFMA32(kf, qf, cneg); SBAR(); \
        kf = lds_rd128(kq + ((u) + 2) * 1024); SBAR(); \
        EX(SC, 0) EX(SC, 1) EX(SC, 2) EX(SC, 3) \
        o = MFMA32(v1, __builtin_bit_cast(bf16x8, pb), o); SBAR(); \
        v1 = lds_rd128(vp + (u) * 1024 + 512); SBAR(); \
        CV(pa, SC, 0) EX(SC, 4) EX(SC, 5) CV(pa, SC, 1) EX(SC, 6) EX(SC, 7) CV(pa, SC, 2) EX(SC, 8) EX(SC, 9) CV(pa, SC, 3) EX(SC, 10) EX(SC, 11) \
        o = MFMA32(v0, __builtin_bit_cast(bf16x8, pa), o); SBAR(); \
        v0 = lds_rd128(vp + ((u) + 1) * 1024); SBAR(); \
        CV(pb, SC, 4) EX(SC, 12) EX(SC, 13) CV(pb, SC, 5) EX(SC, 14) EX(SC, 15) CV(pb, SC, 6) CV(pb, SC, 7)
    lds_cptr kq = kp;
    auto landed = [&](unsigned p) {
#pragma unroll 1
        for (unsigned spin = 0; spin < (1u << 20); ++spin) {
            const u32x4 v = *(LDSP volatile const u32x4*)((lds_ptr)lds + AB_LAND);
            if (__builtin_amdgcn_readfirstlane(min(min(v[0], v[1]), min(v[2], v[3]))) >= p) break;
            __builtin_amdgcn_s_sleep(1);
        }
        asm volatile("" ::: "memory");
    };
    __builtin_amdgcn_s_setprio(2);
#pragma unroll 1
    for (int m = 0; m < 16; ++m) {
        if (__builtin_expect((0xAEAC >> m) & 1, 0)) {
            if (m == 2) {
                STAMP(1, 0);
                landed(1);
                STAMP(1, 1);
                s0 = MFMA32(lds_rd128(kq), qf, cneg); kf = lds_rd128(kq + 1024); v0 = lds_rd128(vp);
            } else if (m == 10) __builtin_amdgcn_s_setprio(1);
            else if (m == 15) __builtin_amdgcn_s_setprio(0);
            else landed((m + 1) >> 1);
        }
        SBAR();
        STEP(s0, s1, 0) STEP(s1, s0, 1) STEP(s0, s1, 2) STEP(s1, s0, 3)
        kq += 4096; vp += vinc;
    }
#undef STEP
#undef EX
#undef CV
    o = MFMA32(v1, __builtin_bit_cast(bf16x8, pb), o);
    STAMP(1, 2);
    float l = o[8];
    float chk = 0.f;
#pragma unroll
    for (int i = 0; i < 8; ++i) chk += fabsf(o[i]);
    const bool bad = !(l < 1.152921504606847e18f) || !(chk < 3.0e38f);
    if (__any(bad)) {
        float m = -INFINITY;
        o = f32x16{};
        lds_cptr vq = isv ? L + AB_V + h * 256 + r * 16 : L + AB_ONES;
        for (int t = 0; t < 64; ++t) {
            f32x16 s = MFMA32(lds_rd128(kp + t * 1024), qf, zero);
            const float mn = fmaxf(m, max16(s));
            const float f = __builtin_amdgcn_exp2f(m - mn);
            m = mn;
#pragma unroll
            for (int i = 0; i < 16; ++i) { s[i] -= mn; o[i] *= f; }
            bf16x8 p0, p1;
            exp_pack(s, p0, p1);
            lds_cptr vt = vq + (isv ? t * 1024 : 0);
            o = MFMA32(lds_rd128(vt), p0, o); o = MFMA32(lds_rd128(vt + 512), p1, o);
        }
        l = o[8];
    }
    const float inv = 1.f / l;
    u32x4 ov;
#pragma unroll
    for (int i = 0; i < 4; ++i) ov[i] = cvtpk(o[2 * i] * inv, o[2 * i + 1] * inv);
    *(u32x4*)(ws + WS_O + (size_t)((8 * chunk + w) * 32 + head) * 1024 + lane * 16) = ov;
    STAMP(1, 3);
}

constexpr int K3_LDS = 8 * 16384;

__global__ __launch_bounds__(256) void k3_out(const u16* __restrict__ x, const u16* __restrict__ wo, char* __restrict__ ws, u16* __restrict__ out) {
    __shared__ __attribute__((aligned(16))) char lds[K3_LDS];
    const int tid = threadIdx.x, lane = tid & 63, w = __builtin_amdgcn_readfirstlane(tid >> 6);
    const int b = blockIdx.x, xcd = b & 7, loc = b >> 3, tp = xcd * 4 + (loc & 3), ct = loc >> 2;
    const int r = lane & 31, h = lane >> 5;
    const unsigned lds0 = lds_addr(lds);

    const int tt = w & 1, g = w >> 1;
    const int token = 64 * tp + 32 * tt + r, col = 64 * ct + 32 * g + 8 * h;
    u32x4 x0, x1, y0, y1, y2, y3; unsigned gtb, key;
    asm volatile("global_load_dwordx4 %0, %8, %9\n\tglobal_load_dwordx4 %1, %8, %9 offset:32\n\t"
                 "global_load_dwordx4 %2, %10, %11\n\tglobal_load_dwordx4 %3, %10, %11 offset:16\n\tglobal_load_dwordx4 %4, %10, %11 offset:64\n\tglobal_load_dwordx4 %5, %10, %11 offset:80\n\t"
                 "global_load_dword %6, %12, %13\n\tglobal_load_dword %7, %14, %15"
                 : "=&v"(x0), "=&v"(x1), "=&v"(y0), "=&v"(y1), "=&v"(y2), "=&v"(y3), "=&v"(gtb), "=&v"(key)
                 : "v"((token * DM + col) * 2), "s"(x), "v"(col * 4), "s"(ws + WS_Y2), "v"(token * 4), "s"(ws + WS_GATE), "v"(lane * 4), "s"(ws + WS_KEYS) : "memory");
    const char* ob = ws + WS_O + (size_t)(2 * tp + (w >> 1)) * 32768 + (w & 1) * 2048;
    const char* wb = (const char*)wo + (size_t)((w & 1) * 32) * 1024 + ct * 128 + (w >> 1) * 64;
    const unsigned ovo = lane * 16, wvo = (lane >> 2) * 1024 + (lane & 3) * 16;
#pragma unroll
    for (int s = 0; s < 8; ++s) {
        const unsigned sl = lds0 + s * 16384;
#pragma unroll
        for (int q = 0; q < 2; ++q) glds16(ovo, ob + s * 4096 + q * 1024, sl + (2 * w + q) * 1024);
#pragma unroll
        for (int q = 0; q < 2; ++q) glds16(wvo, wb + (size_t)(64 * s + 16 * q) * 1024, sl + 8192 + (2 * w + q) * 1024);
    }
    const int blk = (lane >> 4) & 1, q4 = (lane & 15) >> 2, p4 = lane & 3;
    const unsigned wq = 8192 + g * 4096 + (8 * h + q4) * 64 + blk * 32 + (((p4 & 1) << 1) | (p4 >> 1)) * 8;
    const unsigned oo = tt * 4096 + lane * 16;
    lds_cptr L = (lds_cptr)lds;
    f32x16 acc = {};
#define K3_STEP(s, N) WAIT_BAR(N); { _Pragma("unroll") for (int ks = 0; ks < 4; ++ks) { bf16x8 of = lds_rd128(L + (s) * 16384 + oo + ks * 1024); bf16x8 wf = lds_tr2(L + (s) * 16384 + wq + ks * 1024); acc = MFMA32(wf, of, acc); } } SBAR();
    K3_STEP(0, 28)
    asm volatile("" : "+v"(x0), "+v"(x1), "+v"(y0), "+v"(y1), "+v"(y2), "+v"(y3), "+v"(gtb), "+v"(key));
    K3_STEP(1, 24) K3_STEP(2, 20) K3_STEP(3, 16) K3_STEP(4, 12) K3_STEP(5, 8) K3_STEP(6, 4) K3_STEP(7, 0)
#undef K3_STEP
#pragma unroll
    for (int o = 32; o >= 1; o >>= 1) { const unsigned t = __shfl_xor(key, o); key = t > key ? t : key; }
    const bool sel = token == (int)(0xFFFF - (key & 0xFFFF));
    const float gt = __uint_as_float(gtb), omg = rbf(1.f - gt);
    float ysel[16];
#pragma unroll
    for (int i = 0; i < 4; ++i) { ysel[i] = sel ? __uint_as_float(y0[i]) : 0.f; ysel[4 + i] = sel ? __uint_as_float(y1[i]) : 0.f; ysel[8 + i] = sel ? __uint_as_float(y2[i]) : 0.f; ysel[12 + i] = sel ? __uint_as_float(y3[i]) : 0.f; }
#pragma unroll
    for (int e = 0; e < 2; ++e) {
        const u32x4 xv = e ? x1 : x0;
        u32x4 ov;
#pragma unroll
        for (int i = 0; i < 4; ++i) {
            float o2[2];
#pragma unroll
            for (int j = 0; j < 2; ++j) {
                const float xx = j ? __uint_as_float(xv[i] & 0xFFFF0000u) : __uint_as_float(xv[i] << 16);
                const float bb = rbf(xx + ysel[8 * e + 2 * i + j]);
                const float a = rbf(acc[8 * e + 2 * i + j]);
                o2[j] = rbf(gt * a) + rbf(omg * bb);
            }
            ov[i] = cvtpk(o2[0], o2[1]);
        }
        *(u32x4*)(out + token * DM + col + 16 * e) = ov;
    }

}

extern "C" void kernel_launch(void* const* d_in, const int* in_sizes, int n_in, void* d_out, int out_size, void* d_ws, size_t ws_size, hipStream_t stream) {
    const u16* x = (const u16*)d_in[0];
    const u16* wqkv = (const u16*)d_in[1];
    const u16* wo = (const u16*)d_in[2];
    const u16* w_r = (const u16*)d_in[4];
    const u16* wg = (const u16*)d_in[5];
    const u16* wu = (const u16*)d_in[6];
    const u16* wd = (const u16*)d_in[7];
    const u16* gate_w = (const u16*)d_in[8];
    char* ws = (char*)d_ws;
    kab<<<256, 768, 0, stream>>>(x, wqkv, w_r, gate_w, wg, wu, wd, ws);
    k3_out<<<256, 256, 0, stream>>>(x, wo, ws, (u16*)d_out);
}
```

```cpp
#include <hip/hip_runtime.h>
#include <hip/hip_bf16.h>
#include <stdint.h>

typedef __bf16 bf16x8 __attribute__((ext_vector_type(8)));
typedef __bf16 bf16x2 __attribute__((ext_vector_type(2)));
typedef short s16x4 __attribute__((ext_vector_type(4)));
typedef short s16x8 __attribute__((ext_vector_type(8)));
typedef float f32x16 __attribute__((ext_vector_type(16)));
typedef float f32x4 __attribute__((ext_vector_type(4)));
typedef float f32x2 __attribute__((ext_vector_type(2)));
typedef unsigned u32x4 __attribute__((ext_vector_type(4)));
typedef unsigned short u16;
#define LDSP __attribute__((address_space(3)))
typedef LDSP const char* lds_cptr;
typedef LDSP char* lds_ptr;
#define DI __device__ __forceinline__
#define MFMA32(a, b, c) __builtin_amdgcn_mfma_f32_32x32x16_bf16(a, b, c, 0, 0, 0)
#define SBAR() __builtin_amdgcn_sched_barrier(0)

#define MAPPING 0
#define STAMP(kid, i)
constexpr int SEQ = 2048, DM = 512, NH = 32, FF = 1376;
constexpr float QSCALE = 0.25f * 1.4426950408889634f;

constexpr size_t WS_Q = 0, WS_K = 2u << 20, WS_V = 4u << 20, WS_O = 6u << 20;
constexpr size_t WS_GATE = 8u << 20;
constexpr size_t WS_KEYS = WS_GATE + 8192;
constexpr size_t WS_H = WS_KEYS + 1024;
constexpr size_t WS_YP = WS_H + 8192;
constexpr size_t WS_EPOCH = 9u << 20;
constexpr size_t WS_FLAG_A = WS_EPOCH + 4096;
constexpr size_t WS_FLAG_F = WS_FLAG_A + 32768;
constexpr size_t WS_Y2 = WS_FLAG_F + 16384;
constexpr size_t WS_FLAG_Y = WS_Y2 + 4096;
constexpr size_t WS_FLAG_O = WS_FLAG_Y + 4096;

DI unsigned cvtpk(float lo, float hi) { f32x2 v = {lo, hi}; bf16x2 b = __builtin_convertvector(v, bf16x2); return __builtin_bit_cast(unsigned, b); }
DI float bf2f(unsigned b) { return __uint_as_float(b << 16); }
DI float rbf(float x) { return __uint_as_float(cvtpk(x, 0.f) << 16); }
DI unsigned lds_addr(const void* p) { return (unsigned)(size_t)(LDSP const char*)p; }
DI void glds16(unsigned voff, const void* sbase, unsigned ldsdst) {
    asm volatile("s_mov_b32 m0, %2\n\ts_nop 0\n\tglobal_load_lds_dwordx4 %0, %1" ::"v"(voff), "s"(sbase), "s"(ldsdst) : "memory");
}
DI bf16x8 lds_rd128(lds_cptr p) { return *(LDSP const bf16x8*)p; }
DI s16x4 lds_tr(lds_cptr p) { return __builtin_bit_cast(s16x4, __builtin_amdgcn_ds_read_tr16_b64_v4i16((LDSP s16x4*)p)); }
DI bf16x8 lds_tr2(lds_cptr p) {
    s16x4 a = lds_tr(p), b = lds_tr(p + 256);
    s16x8 r = {a[0], a[1], a[2], a[3], b[0], b[1], b[2], b[3]};
    return __builtin_bit_cast(bf16x8, r);
}
#define WAIT_BAR(N) asm volatile("s_waitcnt vmcnt(" #N ") lgkmcnt(0)\n\ts_barrier" ::: "memory")
DI int swap23(int v) { return (v & ~12) | ((v & 4) << 1) | ((v & 8) >> 1); }

constexpr int AB_SLOT = 39168, AB_W = 32768, AB_AUX = 4 * AB_SLOT;
constexpr int AB_K = 0, AB_V = 65536, AB_ONES = 131072;
constexpr int AB_FS = AB_ONES + 5632, AB_FH = AB_FS + 1024, AB_FC = AB_FH + 64;
constexpr int AB_LAND = AB_FC + 16;
constexpr int AB_PROG = AB_LAND + 16;
constexpr int AB_LANDC = AB_FC + 64, AB_KVDONE = AB_FC + 80, AB_ODONE = AB_FC + 84;
constexpr int AB_XCH = AB_FS + 1536;
constexpr int AB_FW = AB_FS + 1280;
constexpr int AB_LDS = AB_AUX + 4096;
static_assert(AB_LDS <= 163840 && AB_FW + 16384 <= AB_LDS && AB_XCH + 16384 <= AB_LDS && AB_FW % 256 == 0, "LDS");

DI float max16(const f32x16& s) {
    float a = fmaxf(fmaxf(s[0], s[1]), s[2]), b = fmaxf(fmaxf(s[3], s[4]), s[5]);
    a = fmaxf(fmaxf(a, s[6]), s[7]); b = fmaxf(fmaxf(b, s[8]), s[9]);
    a = fmaxf(fmaxf(a, s[10]), s[11]); b = fmaxf(fmaxf(b, s[12]), s[13]);
    a = fmaxf(fmaxf(a, s[14]), s[15]);
    float m = fmaxf(a, b);
    auto rr = __builtin_amdgcn_permlane32_swap(__float_as_uint(m), __float_as_uint(m), false, false);
    return fmaxf(__uint_as_float(rr[0]), __uint_as_float(rr[1]));
}
DI void exp_pack(const f32x16& s, bf16x8& p0, bf16x8& p1) {
    u32x4 a, b;
#pragma unroll
    for (int i = 0; i < 4; ++i) a[i] = cvtpk(__builtin_amdgcn_exp2f(s[2 * i]), __builtin_amdgcn_exp2f(s[2 * i + 1]));
#pragma unroll
    for (int i = 0; i < 4; ++i) b[i] = cvtpk(__builtin_amdgcn_exp2f(s[8 + 2 * i]), __builtin_amdgcn_exp2f(s[8 + 2 * i + 1]));
    p0 = __builtin_bit_cast(bf16x8, a); p1 = __builtin_bit_cast(bf16x8, b);
}
DI void st16_dev(unsigned voff, u32x4 d, void* sbase) { asm volatile("global_store_dwordx4 %0, %1, %2 sc1\n\ts_nop 1" ::"v"(voff), "v"(d), "s"(sbase) : "memory"); }
DI void glds16_dev(unsigned voff, const void* sbase, unsigned ldsdst) {
    asm volatile("s_mov_b32 m0, %2\n\ts_nop 0\n\tglobal_load_lds_dwordx4 %0, %1 sc1" ::"v"(voff), "s"(sbase), "s"(ldsdst) : "memory");
}
typedef unsigned long long u64;
DI u64 ld_dev(const void* p) { return __hip_atomic_load((const u64*)p, __ATOMIC_RELAXED, __HIP_MEMORY_SCOPE_AGENT); }
DI void st_dev(void* p, u64 v) { __hip_atomic_store((u64*)p, v, __ATOMIC_RELAXED, __HIP_MEMORY_SCOPE_AGENT); }
DI void wait_flags(const char* flag, bool want, u64 tag) {
#pragma unroll 1
    for (unsigned spin = 0; spin < (1u << 20); ++spin) {
        const u64 v = want ? ld_dev(flag) : tag;
        if (__all(v == tag)) break;
        __builtin_amdgcn_s_sleep(1);
    }
}

#define BARRIER() asm volatile("s_barrier" ::: "memory")
#define WAIT_V(N) asm volatile("s_waitcnt vmcnt(" #N ")" ::: "memory")

DI f32x4 mfma16(bf16x8 a, bf16x8 b, f32x4 c) { return __builtin_amdgcn_mfma_f32_16x16x32_bf16(a, b, c, 0, 0, 0); }
DI void lds_count(LDSP unsigned* c) { asm volatile("s_waitcnt vmcnt(0) lgkmcnt(0)" ::: "memory"); if ((threadIdx.x & 63) == 0) __hip_atomic_fetch_add(c, 1u, __ATOMIC_RELAXED, __HIP_MEMORY_SCOPE_WORKGROUP); }
DI void lds_wait(LDSP unsigned* c, unsigned n) {
#pragma unroll 1
    for (unsigned spin = 0; spin < (1u << 20); ++spin) {
        if (__hip_atomic_load(c, __ATOMIC_RELAXED, __HIP_MEMORY_SCOPE_WORKGROUP) >= n) break;
        __builtin_amdgcn_s_sleep(1);
    }
    asm volatile("" ::: "memory");
}
DI void lds_wait_slow(LDSP unsigned* c, unsigned n) {
#pragma unroll 1
    for (unsigned spin = 0; spin < (1u << 20); ++spin) {
        if (__hip_atomic_load(c, __ATOMIC_RELAXED, __HIP_MEMORY_SCOPE_WORKGROUP) >= n) break;
        __builtin_amdgcn_s_sleep(4);
    }
    asm volatile("" ::: "memory");
}
DI unsigned top_key_dev(char* ws, int lane, u64 tag) {
    wait_flags(ws + WS_FLAG_A + lane * 128, true, tag);
    unsigned key = __hip_atomic_load((const unsigned*)(ws + WS_KEYS) + lane, __ATOMIC_RELAXED, __HIP_MEMORY_SCOPE_AGENT);
#pragma unroll
    for (int o = 32; o >= 1; o >>= 1) { const unsigned t = __shfl_xor(key, o); key = t > key ? t : key; }
    return __builtin_amdgcn_readfirstlane(key);
}
DI bf16x8 tr_frag(lds_cptr p, int step) { s16x4 a = lds_tr(p), c = lds_tr(p + step); s16x8 t = {a[0], a[1], a[2], a[3], c[0], c[1], c[2], c[3]}; return __builtin_bit_cast(bf16x8, t); }
DI void ffn_unit(char* lds, const u16* x, const u16* wg, const u16* wu, const u16* wd, char* ws, int u, int j, int lane, u64 tag) {
    const int n = lane & 15, g = lane >> 4, q = n >> 2, p = n & 3;
    const unsigned slot = lds_addr(lds) + AB_FW + j * 4096;
    lds_cptr sl = (lds_cptr)lds + AB_FW + j * 4096;
    bf16x8 ag[4], au[4], ad[8];
    const unsigned vo = (lane >> 1) * (FF * 2) + (lane & 1) * 16;
    const size_t wo = (size_t)(128 * j) * (FF * 2) + 32 * u;
#pragma unroll
    for (int i = 0; i < 4; ++i) glds16(vo, (const char*)wg + wo + (size_t)i * 32 * (FF * 2), slot + i * 1024);
    asm volatile("s_waitcnt vmcnt(0)" ::: "memory");
#pragma unroll
    for (int ks = 0; ks < 4; ++ks) ag[ks] = tr_frag(sl + (32 * ks + 8 * g + q) * 32 + p * 8, 128);
    asm volatile("s_waitcnt lgkmcnt(0)" : "+v"(ag[0]), "+v"(ag[1]), "+v"(ag[2]), "+v"(ag[3])::"memory");
#pragma unroll
    for (int i = 0; i < 4; ++i) glds16(vo, (const char*)wu + wo + (size_t)i * 32 * (FF * 2), slot + i * 1024);
    asm volatile("s_waitcnt vmcnt(0)" ::: "memory");
#pragma unroll
    for (int ks = 0; ks < 4; ++ks) au[ks] = tr_frag(sl + (32 * ks + 8 * g + q) * 32 + p * 8, 128);
    asm volatile("s_waitcnt lgkmcnt(0)" : "+v"(au[0]), "+v"(au[1]), "+v"(au[2]), "+v"(au[3])::"memory");
#pragma unroll
    for (int i = 0; i < 4; ++i) glds16((lane >> 4) * 1024 + (lane & 15) * 16, (const char*)wd + (size_t)(16 * u + 4 * i) * 1024 + j * 256, slot + i * 1024);
    const unsigned key = top_key_dev(ws, lane, tag);
    const int idx = 0xFFFF - (key & 0xFFFF);
    bf16x8 xs[4];
#pragma unroll
    for (int ks = 0; ks < 4; ++ks) xs[ks] = *(const bf16x8*)(x + idx * DM + 128 * j + 32 * ks + 8 * g);
    asm volatile("s_waitcnt vmcnt(0)" ::: "memory");
#pragma unroll
    for (int t = 0; t < 8; ++t) ad[t] = tr_frag(sl + (8 * (g & 1) + q) * 256 + t * 32 + p * 8, 1024);
    f32x4 dg = {}, du = {};
#pragma unroll
    for (int ks = 0; ks < 4; ++ks) { dg = mfma16(ag[ks], xs[ks], dg); du = mfma16(au[ks], xs[ks], du); }
    LDSP float* fs = (LDSP float*)((lds_ptr)lds + AB_FS);
    LDSP unsigned* fc = (LDSP unsigned*)((lds_ptr)lds + AB_FC);
    if (n == 0) { *(LDSP f32x4*)(fs + j * 32 + 4 * g) = dg; *(LDSP f32x4*)(fs + j * 32 + 16 + 4 * g) = du; }
    lds_count(fc);
    if (j == 0) {
        lds_wait(fc, 4);
        const int l32 = lane & 31;
        const float v = rbf((fs[l32] + fs[32 + l32]) + (fs[64 + l32] + fs[96 + l32]));
        const float up = __shfl(v, (lane & 15) + 16);
        const float hv = rbf(v / (1.f + __expf(-v))) * up;
        if (lane < 16) *(LDSP u16*)((lds_ptr)lds + AB_FH + lane * 2) = (u16)(cvtpk(hv, 0.f) & 0xFFFFu);
        lds_count(fc + 1);
    }
    lds_wait(fc + 1, 1);
    u32x4 hb = *(LDSP const u32x4*)((lds_ptr)lds + AB_FH + (g & 1) * 16);
    if (g >= 2) hb = u32x4{0u, 0u, 0u, 0u};
    char* yb = ws + WS_YP + (size_t)u * 2048 + j * 512;
    f32x4 y[8];
#pragma unroll
    for (int t = 0; t < 8; ++t) y[t] = mfma16(ad[t], __builtin_bit_cast(bf16x8, hb), f32x4{});
    asm volatile("s_nop 7\n\ts_nop 1" : "+v"(y[0]), "+v"(y[1]), "+v"(y[2]), "+v"(y[3]), "+v"(y[4]), "+v"(y[5]), "+v"(y[6]), "+v"(y[7]));
    if (n == 0) {
#pragma unroll
        for (int t = 0; t < 8; ++t) st16_dev(t * 64 + g * 16, __builtin_bit_cast(u32x4, y[t]), yb);
    }
    lds_count(fc + 2);
    if (j == 0) { lds_wait(fc + 2, 4); if (lane == 0) st_dev(ws + WS_FLAG_F + u * 128, tag); }
}
DI void ffn_reduce(char* lds, char* ws, int g8, int j, int lane, u64 tag) {
    const unsigned key = top_key_dev(ws, lane, tag);
    wait_flags(ws + WS_FLAG_F + lane * 128, true, tag);
    wait_flags(ws + WS_FLAG_F + (64 + lane) * 128, lane < 22, tag);
    const unsigned* yp = (const unsigned*)(ws + WS_YP) + 64 * g8 + lane;
    unsigned v[22];
#pragma unroll
    for (int i = 0; i < 22; ++i) { const int u = min(j + 4 * i, 85); v[i] = __hip_atomic_load(yp + u * DM, __ATOMIC_RELAXED, __HIP_MEMORY_SCOPE_AGENT); }
    float y = 0.f;
#pragma unroll
    for (int i = 0; i < 22; ++i) y += (j + 4 * i < 86) ? __uint_as_float(v[i]) : 0.f;
    LDSP float* fs = (LDSP float*)((lds_ptr)lds + AB_FS);
    LDSP unsigned* fc = (LDSP unsigned*)((lds_ptr)lds + AB_FC);
    fs[j * 64 + lane] = y;
    lds_count(fc);
    if (j == 0) {
        lds_wait(fc, 4);
        const float ys = rbf((fs[lane] + fs[64 + lane]) + (fs[128 + lane] + fs[192 + lane]));
        const unsigned ob16 = key >> 16, rb = (ob16 & 0x8000u) ? (ob16 & 0x7FFFu) : (~ob16 & 0xFFFFu);
        const float rs = rbf(1.f / (1.f + __expf(-bf2f(rb))));
        __hip_atomic_store((unsigned*)(ws + WS_Y2) + 64 * g8 + lane, __float_as_uint(rbf(ys * rs)), __ATOMIC_RELAXED, __HIP_MEMORY_SCOPE_AGENT);
        if (lane == 0) __hip_atomic_store((unsigned*)(ws + WS_Y2) + 512 + g8, 0xFFFFu - (key & 0xFFFFu), __ATOMIC_RELAXED, __HIP_MEMORY_SCOPE_AGENT);
        asm volatile("s_waitcnt vmcnt(0)" ::: "memory");
        if (lane == 0) st_dev(ws + WS_FLAG_Y + g8 * 128, tag);
    }
}

DI void ab_loader(char* lds, const u16* x, const u16* wqkv, const u16* w_r, const u16* gate_w, const u16* wg, const u16* wu, const u16* wd, const u16* wo, char* ws, int head, int chunk, int j, int lane) {
    const unsigned lds0 = lds_addr(lds);
    const int rot = (head + chunk) & 7, t = 64 * j + lane;
    __builtin_amdgcn_s_setprio(3);
    unsigned e_lo, e_hi, a0, a1, a2, a3;
    asm volatile("global_load_dword %0, %6, %7 sc1\n\tglobal_load_dword %1, %6, %7 offset:4 sc1\n\t"
                 "global_load_ushort %2, %8, %9\n\tglobal_load_ushort %3, %8, %10\n\tglobal_load_ushort %4, %8, %9 offset:512\n\tglobal_load_ushort %5, %8, %10 offset:512"
                 : "=&v"(e_lo), "=&v"(e_hi), "=&v"(a0), "=&v"(a1), "=&v"(a2), "=&v"(a3) : "v"(0u), "s"(ws + WS_EPOCH), "v"(t * 2), "s"(w_r), "s"(gate_w) : "memory");
    const unsigned xv0 = (lane >> 3) * 1024 + ((lane & 7) ^ ((lane >> 4) & 7)) * 16;
    const unsigned xv1 = (lane >> 3) * 1024 + ((lane & 7) ^ (((lane >> 4) + 4) & 7)) * 16;
    unsigned wsrc[2];
#pragma unroll
    for (int e = 0; e < 2; ++e) {
        const int o = 768 * (2 * j + e) + 16 * lane;
        if (o < 4096) { const int cc = (o >> 4) & 3; wsrc[e] = (o >> 6) * 3072 + (cc < 2 ? 16 * cc : 1024 + 16 * (cc - 2)); }
        else { const int o2 = o - 4096, row = min(o2 >> 5, 63); wsrc[e] = row * 3072 + 2048 + 16 * ((o2 >> 4) & 1); }
    }
    const char* xb = (const char*)x + (size_t)(256 * chunk + 64 * j) * 1024;
    const char* wb = (const char*)wqkv + head * 32;
    auto issue = [&](int i) {
        const int s = (rot + i) & 7;
        const unsigned sl = lds0 + (i & 3) * AB_SLOT;
        if (lane < 48) { glds16(wsrc[0], wb + (size_t)s * 64 * 3072, sl + AB_W + 768 * (2 * j)); glds16(wsrc[1], wb + (size_t)s * 64 * 3072, sl + AB_W + 768 * (2 * j + 1)); }
#pragma unroll
        for (int q = 0; q < 8; ++q) glds16((q & 1) ? xv1 : xv0, xb + q * 8192 + s * 128, sl + (8 * j + q) * 1024);
    };
    STAMP(3, 1); issue(0); STAMP(3, 2); issue(1); STAMP(3, 3);
    asm volatile("s_waitcnt vmcnt(10)" : "+v"(e_lo), "+v"(e_hi), "+v"(a0), "+v"(a1), "+v"(a2), "+v"(a3)::"memory");
    *(LDSP unsigned*)((lds_ptr)lds + AB_AUX + t * 8) = a0 | (a1 << 16);
    *(LDSP unsigned*)((lds_ptr)lds + AB_AUX + (t + 256) * 8) = a2 | (a3 << 16);
    const u64 tag = (((u64)__builtin_amdgcn_readfirstlane(e_hi) << 32) | __builtin_amdgcn_readfirstlane(e_lo)) + 1;
    STAMP(3, 4);
    WAIT_BAR(10); issue(2); STAMP(3, 5);
    WAIT_BAR(10); STAMP(3, 6); issue(3); STAMP(3, 7);
    WAIT_BAR(10); issue(4);
    WAIT_BAR(10); issue(5);
    WAIT_BAR(10); issue(6);
    WAIT_BAR(10); issue(7);
    WAIT_BAR(10);
    WAIT_BAR(0);
    BARRIER();
    if (j == 0 && lane < 32) *(LDSP unsigned*)((lds_ptr)lds + AB_FC + lane * 4) = 0u;
    asm volatile("s_waitcnt lgkmcnt(0)\n\ts_barrier" ::: "memory");
    if (j == 0 && lane == 0) st_dev(ws + WS_FLAG_A + (head * 8 + chunk) * 128, tag);
    wait_flags(ws + WS_FLAG_A + (head * 8 + (lane & 7)) * 128, lane < 8, tag);
    STAMP(2, 0);
    {
        const char* kb = ws + WS_K + (size_t)head * 65536 + j * 2048;
        const char* vb = ws + WS_V + (size_t)head * 65536 + j * 2048;
        auto fetch = [&](int p) {
            const int cp = (chunk + p) & 7;
            glds16_dev(lane * 16, kb + cp * 8192, lds0 + AB_K + p * 8192 + j * 2048); glds16_dev(lane * 16, kb + cp * 8192 + 1024, lds0 + AB_K + p * 8192 + j * 2048 + 1024);
            glds16_dev(lane * 16, vb + cp * 8192, lds0 + AB_V + p * 8192 + j * 2048); glds16_dev(lane * 16, vb + cp * 8192 + 1024, lds0 + AB_V + p * 8192 + j * 2048 + 1024);
        };
        LDSP volatile unsigned* land = (LDSP volatile unsigned*)((lds_ptr)lds + AB_LAND) + j;
#define LANDED(N, p) do { WAIT_V(N); if (lane == 0) *land = p; } while (0)
        fetch(1); fetch(2);
        LANDED(4, 1);
        STAMP(2, 1);
        fetch(3); fetch(4); fetch(5); fetch(6); fetch(7);
        LANDED(20, 2); LANDED(16, 3); LANDED(12, 4); LANDED(8, 5); LANDED(4, 6); LANDED(0, 7);
#undef LANDED
        STAMP(2, 2);
    }
    const int ce = blockIdx.x & 7, tp = 4 * ce + ((blockIdx.x >> 3) & 3), ct = blockIdx.x >> 5;
    const char* wb3 = (const char*)wo + (size_t)((j & 1) * 32) * 1024 + ct * 128 + (j >> 1) * 64;
    const unsigned wvo3 = (lane >> 2) * 1024 + (lane & 3) * 16;
    {
        u32x4 d0, d1, d2, d3;
        const unsigned pv = (128 * ((blockIdx.x >> 3) & 3) + 32 * j + (lane >> 3)) * 1024 + ct * 128 + (lane & 7) * 16;
        asm volatile("global_load_dwordx4 %0, %4, %8\n\tglobal_load_dwordx4 %1, %5, %8\n\tglobal_load_dwordx4 %2, %6, %8\n\tglobal_load_dwordx4 %3, %7, %8\n\ts_waitcnt vmcnt(0)"
                     : "=&v"(d0), "=&v"(d1), "=&v"(d2), "=&v"(d3) : "v"(pv), "v"(pv + 8192), "v"(pv + 16384), "v"(pv + 24576), "s"(wo) : "memory");
    }
    if (blockIdx.x < 86) ffn_unit(lds, x, wg, wu, wd, ws, blockIdx.x, j, lane, tag);
    else if (blockIdx.x < 94) ffn_reduce(lds, ws, blockIdx.x - 86, j, lane, tag);
    STAMP(2, 3);
    if (blockIdx.x == 255 && j == 0) {
#pragma unroll
        for (int i = 0; i < 4; ++i) wait_flags(ws + WS_FLAG_A + (64 * i + lane) * 128, true, tag);
        if (lane == 0) st_dev(ws + WS_EPOCH, tag);
    }
    lds_wait_slow((LDSP unsigned*)((lds_ptr)lds + AB_KVDONE), 8);
#pragma unroll
    for (int s = 0; s < 8; ++s)
#pragma unroll
        for (int q = 0; q < 2; ++q) glds16(wvo3, wb3 + (size_t)(64 * s + 16 * q) * 1024, lds0 + s * 16384 + 8192 + (2 * j + q) * 1024);
    if (j == 0) {
        lds_wait((LDSP unsigned*)((lds_ptr)lds + AB_ODONE), 8);
        if (lane == 0) st_dev(ws + WS_FLAG_O + (head * 8 + chunk) * 128, tag);
    }
    wait_flags(lane < 32 ? ws + WS_FLAG_O + (lane * 8 + ce) * 128 : ws + WS_FLAG_Y + ct * 128, lane <= 32, tag);
    STAMP(2, 4);
    {
        const char* ob = ws + WS_O + (size_t)(2 * tp + (j >> 1)) * 32768 + (j & 1) * 2048;
#pragma unroll
        for (int s = 0; s < 8; ++s)
#pragma unroll
            for (int q = 0; q < 2; ++q) glds16_dev(lane * 16, ob + s * 4096 + q * 1024, lds0 + s * 16384 + (2 * j + q) * 1024);
        LDSP volatile unsigned* land = (LDSP volatile unsigned*)((lds_ptr)lds + AB_LANDC) + j;
#define LANDED(N, p) do { WAIT_V(N); if (lane == 0) *land = p; } while (0)
        LANDED(12, 2); LANDED(8, 4); LANDED(4, 6); LANDED(0, 8);
#undef LANDED
    }
    STAMP(2, 5);
}

__global__ __launch_bounds__(768) void kab(const u16* __restrict__ x, const u16* __restrict__ wqkv, const u16* __restrict__ w_r, const u16* __restrict__ gate_w,
                                          const u16* __restrict__ wg, const u16* __restrict__ wu, const u16* __restrict__ wd, const u16* __restrict__ wo,
                                          char* __restrict__ ws, u16* __restrict__ out) {
    __shared__ __attribute__((aligned(16))) char lds[AB_LDS];
    const int tid = threadIdx.x, lane = tid & 63, w = __builtin_amdgcn_readfirstlane(tid >> 6);
    const int b = blockIdx.x, xcd = b & 7, loc = b >> 3;
    const int head = xcd * 4 + (loc >> 3), chunk = loc & 7;
    if (w >= 8) { ab_loader(lds, x, wqkv, w_r, gate_w, wg, wu, wd, wo, ws, head, chunk, w - 8, lane); return; }
    const int r = lane & 31, h = lane >> 5;
    lds_cptr L = (lds_cptr)lds;
    const int rot = (head + chunk) & 7;
    unsigned xo[4];
#pragma unroll
    for (int ks = 0; ks < 4; ++ks) xo[ks] = (32 * w + r) * 128 + ((2 * ks + h) ^ ((r >> 1) & 7)) * 16;
    const int blk = (lane >> 4) & 1, q4 = (lane & 15) >> 2, p4 = lane & 3;
    const unsigned wq = AB_W + (8 * h + q4) * 64 + blk * 32 + (((p4 & 1) << 1) | (p4 >> 1)) * 8;
    const unsigned wva = blk ? (8 * h + q4) * 8 : (8 * h + q4) * 32 + p4 * 8, wvk = blk ? 128 : 512;
    f32x16 d1 = {}, d2 = {};
    bf16x8 xa[2], qa[2], va[2], xb2[2], qb[2], vb2[2];
    auto rd = [&](int i, int k0, bf16x8 (&xf)[2], bf16x8 (&fq)[2], bf16x8 (&fv)[2]) {
        const int s = (rot + i) & 7;
        lds_cptr sl = L + (i & 3) * AB_SLOT;
        lds_cptr vv = L + wva + (blk ? AB_AUX + s * 512 : (i & 3) * AB_SLOT + AB_W + 4096);
#pragma unroll
        for (int e = 0; e < 2; ++e) {
            const int ks = k0 + e;
            xf[e] = lds_rd128(sl + xo[ks]);
            fq[e] = lds_tr2(sl + wq + ks * 1024);
            s16x4 a = lds_tr(vv + ks * wvk), c = lds_tr(vv + ks * wvk + (wvk >> 2));
            s16x8 t = {a[0], a[1], a[2], a[3], c[0], c[1], c[2], c[3]};
            fv[e] = __builtin_bit_cast(bf16x8, t);
        }
        SBAR();
    };
    auto mm = [&](bf16x8 (&xf)[2], bf16x8 (&fq)[2], bf16x8 (&fv)[2]) {
#pragma unroll
        for (int e = 0; e < 2; ++e) { d1 = MFMA32(fq[e], xf[e], d1); d2 = MFMA32(xf[e], fv[e], d2); }
        SBAR();
    };
#define LBAR() asm volatile("s_waitcnt lgkmcnt(0)\n\ts_barrier" ::: "memory")
    LBAR(); STAMP(0, 0);
    rd(0, 0, xa, qa, va);
#define A_STAGE(i) rd(i, 2, xb2, qb, vb2); mm(xa, qa, va); if ((i) < 7) { LBAR(); rd((i) + 1, 0, xa, qa, va); } mm(xb2, qb, vb2);
    A_STAGE(0) A_STAGE(1) A_STAGE(2) A_STAGE(3) A_STAGE(4) A_STAGE(5) A_STAGE(6) A_STAGE(7)
#undef A_STAGE
    STAMP(0, 1);
    u32x4 qw, kw, vw0, vw1;
#pragma unroll
    for (int i = 0; i < 4; ++i) {
        kw[i] = cvtpk(d1[8 + 2 * i], d1[8 + 2 * i + 1]);
        vw0[i] = cvtpk(d2[2 * i], d2[2 * i + 1]); vw1[i] = cvtpk(d2[8 + 2 * i], d2[8 + 2 * i + 1]);
    }
    const bool isv = r < 16;
    const unsigned vpos = h * 256 + swap23(r & 15) * 16;
    {
        char* kg = ws + WS_K + (size_t)head * 65536 + (8 * chunk + w) * 1024;
        char* vg = ws + WS_V + (size_t)head * 65536 + (8 * chunk + w) * 1024;
        st16_dev(lane * 16, kw, kg);
        if (isv) { st16_dev(vpos, vw0, vg); st16_dev(vpos + 512, vw1, vg); }
    }
#pragma unroll
    for (int i = 0; i < 4; ++i) qw[i] = cvtpk(d1[2 * i] * QSCALE, d1[2 * i + 1] * QSCALE);
    const bf16x8 qf = __builtin_bit_cast(bf16x8, qw);
    if (w == head) {
        LDSP float* rg = (LDSP float*)((lds_ptr)lds + 2 * AB_SLOT + w * 4096);
        if ((r & 30) == 16) {
#pragma unroll
            for (int qd = 0; qd < 4; ++qd) *(LDSP f32x4*)(rg + (r & 1) * 32 + 8 * qd + 4 * h) = f32x4{d2[4 * qd], d2[4 * qd + 1], d2[4 * qd + 2], d2[4 * qd + 3]};
        }
        const int token = 256 * chunk + 32 * w + r;
        const unsigned rb = cvtpk(rg[r], 0.f) & 0xFFFFu;
        unsigned key = (((rb & 0x8000u) ? (~rb & 0xFFFFu) : (rb | 0x8000u)) << 16) | (0xFFFFu - token);
#pragma unroll
        for (int o = 16; o >= 1; o >>= 1) { const unsigned t = __shfl_xor(key, o); key = t > key ? t : key; }
        if (lane == 0) __hip_atomic_store((unsigned*)(ws + WS_KEYS) + 8 * chunk + w, key, __ATOMIC_RELAXED, __HIP_MEMORY_SCOPE_AGENT);
        const float gs = rbf(rg[32 + r]);
        const float g = rbf(1.f / (1.f + __expf(-gs)));
        if (lane < 32) __hip_atomic_store((unsigned*)(ws + WS_GATE) + token, __float_as_uint(g), __ATOMIC_RELAXED, __HIP_MEMORY_SCOPE_AGENT);
    }
    LBAR();
    *(LDSP u32x4*)((lds_ptr)lds + AB_K + w * 1024 + lane * 16) = kw;
    if (isv) { *(LDSP u32x4*)((lds_ptr)lds + AB_V + w * 1024 + vpos) = vw0; *(LDSP u32x4*)((lds_ptr)lds + AB_V + w * 1024 + vpos + 512) = vw1; }
    { u32x4 ones = {0x3F803F80u, 0x3F803F80u, 0x3F803F80u, 0x3F803F80u}; if (tid < 352) *(LDSP u32x4*)((lds_ptr)lds + AB_ONES + tid * 16) = ones; }
    WAIT_BAR(0);
    STAMP(0, 2);
    lds_cptr kp = L + AB_K + lane * 16;
    lds_cptr vp = isv ? L + AB_V + h * 256 + r * 16 : L + AB_ONES + ((r >= 20 && r < 28) ? 64 : 0);
    const unsigned vinc = isv ? 4096u : 0u;
    f32x16 zero = {};
    bf16x8 kf = lds_rd128(kp);
    f32x16 s0 = MFMA32(kf, qf, zero);
    const float mref = max16(s0);
    f32x16 cneg;
#pragma unroll
    for (int i = 0; i < 16; ++i) { cneg[i] = -mref; s0[i] -= mref; }
    f32x16 o = {}, s1;
    kf = lds_rd128(kp + 1024);
    bf16x8 v0 = lds_rd128(vp), v1 = lds_rd128(vp + 512);
    u32x4 pa, pb = {0u, 0u, 0u, 0u};
#define EX(S, i) S[i] = __builtin_amdgcn_exp2f(S[i]); SBAR();
#define CV(P, S, i) P[i & 3] = cvtpk(S[2 * (i)], S[2 * (i) + 1]); SBAR();
#define STEP(SC, SN, u) \
        SN = MFMA32(kf, qf, cneg); SBAR(); \
        kf = lds_rd128(kq + ((u) + 2) * 1024); SBAR(); \
        EX(SC, 0) EX(SC, 1) EX(SC, 2) EX(SC, 3) \
        o = MFMA32(v1, __builtin_bit_cast(bf16x8, pb), o); SBAR(); \
        v1 = lds_rd128(vp + (u) * 1024 + 512); SBAR(); \
        CV(pa, SC, 0) EX(SC, 4) EX(SC, 5) CV(pa, SC, 1) EX(SC, 6) EX(SC, 7) CV(pa, SC, 2) EX(SC, 8) EX(SC, 9) CV(pa, SC, 3) EX(SC, 10) EX(SC, 11) \
        o = MFMA32(v0, __builtin_bit_cast(bf16x8, pa), o); SBAR(); \
        v0 = lds_rd128(vp + ((u) + 1) * 1024); SBAR(); \
        CV(pb, SC, 4) EX(SC, 12) EX(SC, 13) CV(pb, SC, 5) EX(SC, 14) EX(SC, 15) CV(pb, SC, 6) CV(pb, SC, 7)
    lds_cptr kq = kp;
    auto wait_words = [&](int off, unsigned p) -> unsigned {
        unsigned have = 0;
#pragma unroll 1
        for (unsigned spin = 0; spin < (1u << 20); ++spin) {
            const u32x4 v = *(LDSP volatile const u32x4*)((lds_ptr)lds + off);
            have = __builtin_amdgcn_readfirstlane(min(min(v[0], v[1]), min(v[2], v[3])));
            if (have >= p) break;
            __builtin_amdgcn_s_sleep(1);
        }
        asm volatile("" ::: "memory");
        return have;
    };
    unsigned kv_have = 0;
    auto landed = [&](unsigned p) { if (kv_have < p) kv_have = wait_words(AB_LAND, p); };
    LDSP volatile unsigned* prog = (LDSP volatile unsigned*)((lds_ptr)lds + AB_PROG);
    __builtin_amdgcn_s_setprio(1);
#pragma unroll 1
    for (int m = 0; m < 16; ++m) {
        if (__builtin_expect((0x2AAC >> m) & 1, 0)) {
            if (m == 2) {
                STAMP(1, 0);
                landed(1);
                STAMP(1, 1);
                s0 = MFMA32(lds_rd128(kq), qf, cneg); kf = lds_rd128(kq + 1024); v0 = lds_rd128(vp);
            } else landed((m + 1) >> 1);
        }
        prog[w] = m;
        const unsigned other = prog[w ^ 4];
        SBAR();
        STEP(s0, s1, 0) STEP(s1, s0, 1) STEP(s0, s1, 2) STEP(s1, s0, 3)
        kq += 4096; vp += vinc;
        if ((unsigned)m >= __builtin_amdgcn_readfirstlane(other) + (w >> 2)) __builtin_amdgcn_s_setprio(0); else __builtin_amdgcn_s_setprio(1);
    }
#undef STEP
#undef EX
#undef CV
    o = MFMA32(v1, __builtin_bit_cast(bf16x8, pb), o);
    STAMP(1, 2);
    float l = o[8];
    float chk = 0.f;
#pragma unroll
    for (int i = 0; i < 8; ++i) chk += fabsf(o[i]);
    const bool bad = !(l < 1.152921504606847e18f) || !(chk < 3.0e38f);
    if (__any(bad)) {
        float m = -INFINITY;
        o = f32x16{};
        lds_cptr vq = isv ? L + AB_V + h * 256 + r * 16 : L + AB_ONES;
        for (int t = 0; t < 64; ++t) {
            f32x16 s = MFMA32(lds_rd128(kp + t * 1024), qf, zero);
            const float mn = fmaxf(m, max16(s));
            const float f = __builtin_amdgcn_exp2f(m - mn);
            m = mn;
#pragma unroll
            for (int i = 0; i < 16; ++i) { s[i] -= mn; o[i] *= f; }
            bf16x8 p0, p1;
            exp_pack(s, p0, p1);
            lds_cptr vt = vq + (isv ? t * 1024 : 0);
            o = MFMA32(lds_rd128(vt), p0, o); o = MFMA32(lds_rd128(vt + 512), p1, o);
        }
        l = o[8];
    }
    asm volatile("s_waitcnt lgkmcnt(0)" ::: "memory");
    if (lane == 0) __hip_atomic_fetch_add((LDSP unsigned*)((lds_ptr)lds + AB_KVDONE), 1u, __ATOMIC_RELAXED, __HIP_MEMORY_SCOPE_WORKGROUP);
    const float inv = 1.f / l;
    u32x4 ov;
#pragma unroll
    for (int i = 0; i < 4; ++i) ov[i] = cvtpk(o[2 * i] * inv, o[2 * i + 1] * inv);
    st16_dev(lane * 16, ov, ws + WS_O + (size_t)((8 * chunk + w) * 32 + head) * 1024);
    __builtin_amdgcn_s_setprio(0);
    const int tp = 4 * xcd + (loc & 3), ct = loc >> 2;
    const int tt = w & 1, cg = (w >> 1) & 1, kh = w >> 2;
    const int token = 64 * tp + 32 * tt + r, col = 64 * ct + 32 * cg + 16 * kh + 8 * h;
    u32x4 xv;
    asm volatile("global_load_dwordx4 %0, %1, %2" : "=&v"(xv) : "v"((token * DM + col) * 2), "s"(x) : "memory");
    lds_count((LDSP unsigned*)((lds_ptr)lds + AB_ODONE));
    asm volatile("" : "+v"(xv));
    STAMP(1, 3);
    const unsigned wq3 = 8192 + cg * 4096 + (8 * h + q4) * 64 + blk * 32 + (((p4 & 1) << 1) | (p4 >> 1)) * 8 + kh * 2048;
    const unsigned oo3 = tt * 4096 + kh * 2048 + lane * 16;
    f32x16 acc = {};
    u32x4 y0, y1; unsigned gtb, idxb;
#define C_STAGE(s) { _Pragma("unroll") for (int ks = 0; ks < 2; ++ks) { bf16x8 of = lds_rd128(L + (s) * 16384 + oo3 + ks * 1024); bf16x8 wf = lds_tr2(L + (s) * 16384 + wq3 + ks * 1024); acc = MFMA32(wf, of, acc); } }
    unsigned c_have = wait_words(AB_LANDC, 2);
    STAMP(1, 4);
    asm volatile("global_load_dwordx4 %0, %4, %5 sc1\n\tglobal_load_dwordx4 %1, %4, %5 offset:16 sc1\n\tglobal_load_dword %2, %6, %7 sc1\n\tglobal_load_dword %3, %8, %5 offset:2048 sc1"
                 : "=&v"(y0), "=&v"(y1), "=&v"(gtb), "=&v"(idxb) : "v"(col * 4), "s"(ws + WS_Y2), "v"(token * 4), "s"(ws + WS_GATE), "v"(ct * 4) : "memory");
    C_STAGE(0) C_STAGE(1)
    if (c_have < 4) c_have = wait_words(AB_LANDC, 4);
    C_STAGE(2) C_STAGE(3)
    if (c_have < 6) c_have = wait_words(AB_LANDC, 6);
    C_STAGE(4) C_STAGE(5)
    if (c_have < 8) c_have = wait_words(AB_LANDC, 8);
    C_STAGE(6) C_STAGE(7)
#undef C_STAGE
    {
        LDSP f32x4* xw = (LDSP f32x4*)((lds_ptr)lds + AB_XCH + w * 2048) + lane;
        xw[0] = kh ? f32x4{acc[0], acc[1], acc[2], acc[3]} : f32x4{acc[8], acc[9], acc[10], acc[11]};
        xw[64] = kh ? f32x4{acc[4], acc[5], acc[6], acc[7]} : f32x4{acc[12], acc[13], acc[14], acc[15]};
    }
    asm volatile("s_waitcnt vmcnt(0) lgkmcnt(0)\n\ts_barrier" : "+v"(y0), "+v"(y1), "+v"(gtb), "+v"(idxb)::"memory");
    {
        LDSP const f32x4* xr = (LDSP const f32x4*)((lds_ptr)lds + AB_XCH + (w ^ 4) * 2048) + lane;
        const f32x4 pa0 = xr[0], pa1 = xr[64];
        const bool sel = token == (int)idxb;
        const float gt = __uint_as_float(gtb), omg = rbf(1.f - gt);
        u32x4 res;
#pragma unroll
        for (int i = 0; i < 4; ++i) {
            float o2[2];
#pragma unroll
            for (int e = 0; e < 2; ++e) {
                const int c = 2 * i + e;
                const float a = rbf((kh ? acc[8 + c] : acc[c]) + (c < 4 ? pa0[c & 3] : pa1[c & 3]));
                const float xx = e ? __uint_as_float(xv[i] & 0xFFFF0000u) : __uint_as_float(xv[i] << 16);
                const float ys = __uint_as_float(c < 4 ? y0[c & 3] : y1[c & 3]);
                const float bb = rbf(xx + (sel ? ys : 0.f));
                o2[e] = rbf(gt * a) + rbf(omg * bb);
            }
            res[i] = cvtpk(o2[0], o2[1]);
        }
        *(u32x4*)(out + token * DM + col) = res;
    }
    STAMP(1, 5);
}

extern "C" void kernel_launch(void* const* d_in, const int* in_sizes, int n_in, void* d_out, int out_size, void* d_ws, size_t ws_size, hipStream_t stream) {
    const u16* x = (const u16*)d_in[0];
    const u16* wqkv = (const u16*)d_in[1];
    const u16* wo = (const u16*)d_in[2];
    const u16* w_r = (const u16*)d_in[4];
    const u16* wg = (const u16*)d_in[5];
    const u16* wu = (const u16*)d_in[6];
    const u16* wd = (const u16*)d_in[7];
    const u16* gate_w = (const u16*)d_in[8];
    char* ws = (char*)d_ws;
    kab<<<256, 768, 0, stream>>>(x, wqkv, w_r, gate_w, wg, wu, wd, wo, ws, (u16*)d_out);
}
```

```cpp
#include <hip/hip_runtime.h>
#include <hip/hip_bf16.h>
#include <stdint.h>

typedef __bf16 bf16x8 __attribute__((ext_vector_type(8)));
typedef __bf16 bf16x2 __attribute__((ext_vector_type(2)));
typedef short s16x4 __attribute__((ext_vector_type(4)));
typedef short s16x8 __attribute__((ext_vector_type(8)));
typedef float f32x16 __attribute__((ext_vector_type(16)));
typedef float f32x4 __attribute__((ext_vector_type(4)));
typedef float f32x2 __attribute__((ext_vector_type(2)));
typedef unsigned u32x4 __attribute__((ext_vector_type(4)));
typedef unsigned short u16;
#define LDSP __attribute__((address_space(3)))
typedef LDSP const char* lds_cptr;
typedef LDSP char* lds_ptr;
#define DI __device__ __forceinline__
#define MFMA32(a, b, c) __builtin_amdgcn_mfma_f32_32x32x16_bf16(a, b, c, 0, 0, 0)
#define SBAR() __builtin_amdgcn_sched_barrier(0)

#define MAPPING 0
#define STAMP(kid, i)
constexpr int SEQ = 2048, DM = 512, NH = 32, FF = 1376;
constexpr float QSCALE = 0.25f * 1.4426950408889634f;

constexpr size_t WS_Q = 0, WS_K = 2u << 20, WS_V = 4u << 20, WS_O = 6u << 20;
constexpr size_t WS_GATE = 8u << 20;
constexpr size_t WS_KEYS = WS_GATE + 8192;
constexpr size_t WS_H = WS_KEYS + 1024;
constexpr size_t WS_YP = WS_H + 8192;
constexpr size_t WS_EPOCH = 9u << 20;
constexpr size_t WS_FLAG_A = WS_EPOCH + 4096;
constexpr size_t WS_FLAG_F = WS_FLAG_A + 32768;
constexpr size_t WS_Y2 = WS_FLAG_F + 16384;
constexpr size_t WS_FLAG_Y = WS_Y2 + 4096;
constexpr size_t WS_FLAG_O = WS_FLAG_Y + 4096;

DI unsigned cvtpk(float lo, float hi) { f32x2 v = {lo, hi}; bf16x2 b = __builtin_convertvector(v, bf16x2); return __builtin_bit_cast(unsigned, b); }
DI float bf2f(unsigned b) { return __uint_as_float(b << 16); }
DI float rbf(float x) { return __uint_as_float(cvtpk(x, 0.f) << 16); }
DI unsigned lds_addr(const void* p) { return (unsigned)(size_t)(LDSP const char*)p; }
DI void glds16(unsigned voff, const void* sbase, unsigned ldsdst) {
    asm volatile("s_mov_b32 m0, %2\n\ts_nop 0\n\tglobal_load_lds_dwordx4 %0, %1" ::"v"(voff), "s"(sbase), "s"(ldsdst) : "memory");
}
DI bf16x8 lds_rd128(lds_cptr p) { return *(LDSP const bf16x8*)p; }
DI s16x4 lds_tr(lds_cptr p) { return __builtin_bit_cast(s16x4, __builtin_amdgcn_ds_read_tr16_b64_v4i16((LDSP s16x4*)p)); }
DI bf16x8 lds_tr2(lds_cptr p) {
    s16x4 a = lds_tr(p), b = lds_tr(p + 256);
    s16x8 r = {a[0], a[1], a[2], a[3], b[0], b[1], b[2], b[3]};
    return __builtin_bit_cast(bf16x8, r);
}
#define WAIT_BAR(N) asm volatile("s_waitcnt vmcnt(" #N ") lgkmcnt(0)\n\ts_barrier" ::: "memory")
DI int swap23(int v) { return (v & ~12) | ((v & 4) << 1) | ((v & 8) >> 1); }

constexpr int AB_SLOT = 39168, AB_W = 32768, AB_AUX = 4 * AB_SLOT;
constexpr int AB_K = 0, AB_V = 65536, AB_ONES = 131072;
constexpr int AB_FS = AB_ONES + 5632, AB_FH = AB_FS + 1024, AB_FC = AB_FH + 64;
constexpr int AB_XCH_ = AB_FS + 1536;
constexpr int AB_LAND = AB_FC + 16;
constexpr int AB_PROG = AB_LAND + 16;
constexpr int AB_LANDC = AB_FC + 64, AB_KVDONE = AB_FC + 80, AB_ODONE = AB_FC + 84, AB_SEEN = AB_FC + 88;
constexpr int AB_ADONE = AB_FC + 92, AB_SEENA = AB_FC + 96, AB_TAG = AB_FC + 128;
constexpr int AB_RG = AB_XCH_ + 16384;
constexpr int AB_XCH = AB_XCH_;
constexpr int AB_FW = AB_FS + 1280;
constexpr int AB_LDS = AB_AUX + 4096;
static_assert(AB_LDS <= 163840 && AB_FW + 16384 <= AB_LDS && AB_RG + 256 <= AB_AUX && AB_FW % 256 == 0, "LDS");

DI float max16(const f32x16& s) {
    float a = fmaxf(fmaxf(s[0], s[1]), s[2]), b = fmaxf(fmaxf(s[3], s[4]), s[5]);
    a = fmaxf(fmaxf(a, s[6]), s[7]); b = fmaxf(fmaxf(b, s[8]), s[9]);
    a = fmaxf(fmaxf(a, s[10]), s[11]); b = fmaxf(fmaxf(b, s[12]), s[13]);
    a = fmaxf(fmaxf(a, s[14]), s[15]);
    float m = fmaxf(a, b);
    auto rr = __builtin_amdgcn_permlane32_swap(__float_as_uint(m), __float_as_uint(m), false, false);
    return fmaxf(__uint_as_float(rr[0]), __uint_as_float(rr[1]));
}
DI void exp_pack(const f32x16& s, bf16x8& p0, bf16x8& p1) {
    u32x4 a, b;
#pragma unroll
    for (int i = 0; i < 4; ++i) a[i] = cvtpk(__builtin_amdgcn_exp2f(s[2 * i]), __builtin_amdgcn_exp2f(s[2 * i + 1]));
#pragma unroll
    for (int i = 0; i < 4; ++i) b[i] = cvtpk(__builtin_amdgcn_exp2f(s[8 + 2 * i]), __builtin_amdgcn_exp2f(s[8 + 2 * i + 1]));
    p0 = __builtin_bit_cast(bf16x8, a); p1 = __builtin_bit_cast(bf16x8, b);
}
DI void st16_dev(unsigned voff, u32x4 d, void* sbase) { asm volatile("global_store_dwordx4 %0, %1, %2 sc1\n\ts_nop 1" ::"v"(voff), "v"(d), "s"(sbase) : "memory"); }
DI void glds16_dev(unsigned voff, const void* sbase, unsigned ldsdst) {
    asm volatile("s_mov_b32 m0, %2\n\ts_nop 0\n\tglobal_load_lds_dwordx4 %0, %1 sc1" ::"v"(voff), "s"(sbase), "s"(ldsdst) : "memory");
}
typedef unsigned long long u64;
DI u64 ld_dev(const void* p) { return __hip_atomic_load((const u64*)p, __ATOMIC_RELAXED, __HIP_MEMORY_SCOPE_AGENT); }
DI void st_dev(void* p, u64 v) { __hip_atomic_store((u64*)p, v, __ATOMIC_RELAXED, __HIP_MEMORY_SCOPE_AGENT); }
DI void wait_flags(const char* flag, bool want, u64 tag) {
#pragma unroll 1
    for (unsigned spin = 0; spin < (1u << 20); ++spin) {
        const u64 v = want ? ld_dev(flag) : tag;
        if (__all(v == tag)) break;
        __builtin_amdgcn_s_sleep(1);
    }
}

#define BARRIER() asm volatile("s_barrier" ::: "memory")
#define WAIT_V(N) asm volatile("s_waitcnt vmcnt(" #N ")" ::: "memory")

DI f32x4 mfma16(bf16x8 a, bf16x8 b, f32x4 c) { return __builtin_amdgcn_mfma_f32_16x16x32_bf16(a, b, c, 0, 0, 0); }
DI void lds_count(LDSP unsigned* c) { asm volatile("s_waitcnt vmcnt(0) lgkmcnt(0)" ::: "memory"); if ((threadIdx.x & 63) == 0) __hip_atomic_fetch_add(c, 1u, __ATOMIC_RELAXED, __HIP_MEMORY_SCOPE_WORKGROUP); }
DI void lds_wait(LDSP unsigned* c, unsigned n) {
#pragma unroll 1
    for (unsigned spin = 0; spin < (1u << 20); ++spin) {
        if (__hip_atomic_load(c, __ATOMIC_RELAXED, __HIP_MEMORY_SCOPE_WORKGROUP) >= n) break;
        __builtin_amdgcn_s_sleep(1);
    }
    asm volatile("" ::: "memory");
}
DI void lds_wait_slow(LDSP unsigned* c, unsigned n) {
#pragma unroll 1
    for (unsigned spin = 0; spin < (1u << 20); ++spin) {
        if (__hip_atomic_load(c, __ATOMIC_RELAXED, __HIP_MEMORY_SCOPE_WORKGROUP) >= n) break;
        __builtin_amdgcn_s_sleep(4);
    }
    asm volatile("" ::: "memory");
}
DI unsigned top_key_dev(char* ws, int lane, u64 tag) {
    u64 v = 0;
#pragma unroll 1
    for (unsigned spin = 0; spin < (1u << 20); ++spin) {
        v = ld_dev(ws + WS_KEYS + lane * 8);
        if (__all((unsigned)(v >> 32) == (unsigned)tag)) break;
        __builtin_amdgcn_s_sleep(2);
    }
    unsigned key = (unsigned)v;
#pragma unroll
    for (int o = 32; o >= 1; o >>= 1) { const unsigned t = __shfl_xor(key, o); key = t > key ? t : key; }
    return __builtin_amdgcn_readfirstlane(key);
}
DI bf16x8 tr_frag(lds_cptr p, int step) { s16x4 a = lds_tr(p), c = lds_tr(p + step); s16x8 t = {a[0], a[1], a[2], a[3], c[0], c[1], c[2], c[3]}; return __builtin_bit_cast(bf16x8, t); }
DI void ffn_unit(char* lds, const u16* x, const u16* wg, const u16* wu, const u16* wd, char* ws, int u, int j, int lane, u64 tag) {
    const int n = lane & 15, g = lane >> 4, q = n >> 2, p = n & 3;
    const unsigned slot = lds_addr(lds) + AB_FW + j * 4096;
    lds_cptr sl = (lds_cptr)lds + AB_FW + j * 4096;
    bf16x8 ag[4], au[4], ad[8];
    const unsigned vo = (lane >> 1) * (FF * 2) + (lane & 1) * 16;
    const size_t wo = (size_t)(128 * j) * (FF * 2) + 32 * u;
#pragma unroll
    for (int i = 0; i < 4; ++i) glds16(vo, (const char*)wg + wo + (size_t)i * 32 * (FF * 2), slot + i * 1024);
    asm volatile("s_waitcnt vmcnt(0)" ::: "memory");
#pragma unroll
    for (int ks = 0; ks < 4; ++ks) ag[ks] = tr_frag(sl + (32 * ks + 8 * g + q) * 32 + p * 8, 128);
    asm volatile("s_waitcnt lgkmcnt(0)" : "+v"(ag[0]), "+v"(ag[1]), "+v"(ag[2]), "+v"(ag[3])::"memory");
#pragma unroll
    for (int i = 0; i < 4; ++i) glds16(vo, (const char*)wu + wo + (size_t)i * 32 * (FF * 2), slot + i * 1024);
    asm volatile("s_waitcnt vmcnt(0)" ::: "memory");
#pragma unroll
    for (int ks = 0; ks < 4; ++ks) au[ks] = tr_frag(sl + (32 * ks + 8 * g + q) * 32 + p * 8, 128);
    asm volatile("s_waitcnt lgkmcnt(0)" : "+v"(au[0]), "+v"(au[1]), "+v"(au[2]), "+v"(au[3])::"memory");
#pragma unroll
    for (int i = 0; i < 4; ++i) glds16((lane >> 4) * 1024 + (lane & 15) * 16, (const char*)wd + (size_t)(16 * u + 4 * i) * 1024 + j * 256, slot + i * 1024);
    const unsigned key = top_key_dev(ws, lane, tag);
    const int idx = 0xFFFF - (key & 0xFFFF);
    bf16x8 xs[4];
#pragma unroll
    for (int ks = 0; ks < 4; ++ks) xs[ks] = *(const bf16x8*)(x + idx * DM + 128 * j + 32 * ks + 8 * g);
    asm volatile("s_waitcnt vmcnt(0)" ::: "memory");
#pragma unroll
    for (int t = 0; t < 8; ++t) ad[t] = tr_frag(sl + (8 * (g & 1) + q) * 256 + t * 32 + p * 8, 1024);
    f32x4 dg = {}, du = {};
#pragma unroll
    for (int ks = 0; ks < 4; ++ks) { dg = mfma16(ag[ks], xs[ks], dg); du = mfma16(au[ks], xs[ks], du); }
    LDSP float* fs = (LDSP float*)((lds_ptr)lds + AB_FS);
    LDSP unsigned* fc = (LDSP unsigned*)((lds_ptr)lds + AB_FC);
    if (n == 0) { *(LDSP f32x4*)(fs + j * 32 + 4 * g) = dg; *(LDSP f32x4*)(fs + j * 32 + 16 + 4 * g) = du; }
    lds_count(fc);
    if (j == 0) {
        lds_wait(fc, 4);
        const int l32 = lane & 31;
        const float v = rbf((fs[l32] + fs[32 + l32]) + (fs[64 + l32] + fs[96 + l32]));
        const float up = __shfl(v, (lane & 15) + 16);
        const float hv = rbf(v / (1.f + __expf(-v))) * up;
        if (lane < 16) *(LDSP u16*)((lds_ptr)lds + AB_FH + lane * 2) = (u16)(cvtpk(hv, 0.f) & 0xFFFFu);
        lds_count(fc + 1);
    }
    lds_wait(fc + 1, 1);
    u32x4 hb = *(LDSP const u32x4*)((lds_ptr)lds + AB_FH + (g & 1) * 16);
    if (g >= 2) hb = u32x4{0u, 0u, 0u, 0u};
    char* yb = ws + WS_YP + (size_t)u * 2048 + j * 512;
    f32x4 y[8];
#pragma unroll
    for (int t = 0; t < 8; ++t) y[t] = mfma16(ad[t], __builtin_bit_cast(bf16x8, hb), f32x4{});
    asm volatile("s_nop 7\n\ts_nop 1" : "+v"(y[0]), "+v"(y[1]), "+v"(y[2]), "+v"(y[3]), "+v"(y[4]), "+v"(y[5]), "+v"(y[6]), "+v"(y[7]));
    if (n == 0) {
#pragma unroll
        for (int t = 0; t < 8; ++t) st16_dev(t * 64 + g * 16, __builtin_bit_cast(u32x4, y[t]), yb);
    }
    lds_count(fc + 2);
    if (j == 0) { lds_wait(fc + 2, 4); if (lane == 0) st_dev(ws + WS_FLAG_F + u * 128, tag); }
}
DI void ffn_reduce(char* lds, char* ws, int g8, int j, int lane, u64 tag) {
    const unsigned key = top_key_dev(ws, lane, tag);
    wait_flags(ws + WS_FLAG_F + lane * 128, true, tag);
    wait_flags(ws + WS_FLAG_F + (64 + lane) * 128, lane < 22, tag);
    const unsigned* yp = (const unsigned*)(ws + WS_YP) + 64 * g8 + lane;
    unsigned v[22];
#pragma unroll
    for (int i = 0; i < 22; ++i) { const int u = min(j + 4 * i, 85); v[i] = __hip_atomic_load(yp + u * DM, __ATOMIC_RELAXED, __HIP_MEMORY_SCOPE_AGENT); }
    float y = 0.f;
#pragma unroll
    for (int i = 0; i < 22; ++i) y += (j + 4 * i < 86) ? __uint_as_float(v[i]) : 0.f;
    LDSP float* fs = (LDSP float*)((lds_ptr)lds + AB_FS);
    LDSP unsigned* fc = (LDSP unsigned*)((lds_ptr)lds + AB_FC);
    fs[j * 64 + lane] = y;
    lds_count(fc);
    if (j == 0) {
        lds_wait(fc, 4);
        const float ys = rbf((fs[lane] + fs[64 + lane]) + (fs[128 + lane] + fs[192 + lane]));
        const unsigned ob16 = key >> 16, rb = (ob16 & 0x8000u) ? (ob16 & 0x7FFFu) : (~ob16 & 0xFFFFu);
        const float rs = rbf(1.f / (1.f + __expf(-bf2f(rb))));
        __hip_atomic_store((unsigned*)(ws + WS_Y2) + 64 * g8 + lane, __float_as_uint(rbf(ys * rs)), __ATOMIC_RELAXED, __HIP_MEMORY_SCOPE_AGENT);
        if (lane == 0) __hip_atomic_store((unsigned*)(ws + WS_Y2) + 512 + g8, 0xFFFFu - (key & 0xFFFFu), __ATOMIC_RELAXED, __HIP_MEMORY_SCOPE_AGENT);
        asm volatile("s_waitcnt vmcnt(0)" ::: "memory");
        if (lane == 0) st_dev(ws + WS_FLAG_Y + g8 * 128, tag);
    }
}

DI void ab_loader(char* lds, const u16* x, const u16* wqkv, const u16* w_r, const u16* gate_w, const u16* wg, const u16* wu, const u16* wd, const u16* wo, char* ws, int head, int chunk, int j, int lane) {
    const unsigned lds0 = lds_addr(lds);
    const int rot = (head + chunk) & 7;
    __builtin_amdgcn_s_setprio(3);
    const unsigned xv0 = (lane >> 3) * 1024 + ((lane & 7) ^ ((lane >> 4) & 7)) * 16;
    const unsigned xv1 = (lane >> 3) * 1024 + ((lane & 7) ^ (((lane >> 4) + 4) & 7)) * 16;
    unsigned wsrc[2];
#pragma unroll
    for (int e = 0; e < 2; ++e) {
        const int o = 768 * (2 * j + e) + 16 * lane;
        if (o < 4096) { const int cc = (o >> 4) & 3; wsrc[e] = (o >> 6) * 3072 + (cc < 2 ? 16 * cc : 1024 + 16 * (cc - 2)); }
        else { const int o2 = o - 4096, row = min(o2 >> 5, 63); wsrc[e] = row * 3072 + 2048 + 16 * ((o2 >> 4) & 1); }
    }
    const char* xb = (const char*)x + (size_t)(256 * chunk + 64 * j) * 1024;
    const char* wb = (const char*)wqkv + head * 32;
    auto issue = [&](int i) {
        const int s = (rot + i) & 7;
        const unsigned sl = lds0 + (i & 3) * AB_SLOT;
        if (lane < 48) { glds16(wsrc[0], wb + (size_t)s * 64 * 3072, sl + AB_W + 768 * (2 * j)); glds16(wsrc[1], wb + (size_t)s * 64 * 3072, sl + AB_W + 768 * (2 * j + 1)); }
#pragma unroll
        for (int q = 0; q < 8; ++q) glds16((q & 1) ? xv1 : xv0, xb + q * 8192 + s * 128, sl + (8 * j + q) * 1024);
    };
    STAMP(3, 1); issue(0); issue(1);
    unsigned e_lo, e_hi;
    asm volatile("global_load_dword %0, %2, %3 sc1\n\tglobal_load_dword %1, %2, %3 offset:4 sc1" : "=&v"(e_lo), "=&v"(e_hi) : "v"(0u), "s"(ws + WS_EPOCH) : "memory");
    WAIT_BAR(12); STAMP(3, 2); issue(2);
    asm volatile("s_waitcnt vmcnt(10)\n\ts_barrier" : "+v"(e_lo), "+v"(e_hi)::"memory");
    const u64 tag = (((u64)__builtin_amdgcn_readfirstlane(e_hi) << 32) | __builtin_amdgcn_readfirstlane(e_lo)) + 1;
    issue(3);
    WAIT_BAR(10); issue(4);
    WAIT_BAR(10); issue(5);
    WAIT_BAR(10); issue(6);
    WAIT_BAR(10); issue(7);
    WAIT_BAR(10);
    WAIT_BAR(0);
    BARRIER();
    if (j == 0) {
        if (lane < 32) *(LDSP unsigned*)((lds_ptr)lds + AB_FC + lane * 4) = 0u;
        if (lane == 0) *(LDSP u64*)((lds_ptr)lds + AB_TAG) = tag;
    }
    asm volatile("s_waitcnt lgkmcnt(0)\n\ts_barrier" ::: "memory");
    if (j == 0) {
        lds_wait((LDSP unsigned*)((lds_ptr)lds + AB_ADONE), 8);
        if (lane == 0) st_dev(ws + WS_FLAG_A + (head * 8 + chunk) * 128, tag);
        wait_flags(ws + WS_FLAG_A + (head * 8 + (lane & 7)) * 128, lane < 8, tag);
        if (lane == 0) *(LDSP volatile unsigned*)((lds_ptr)lds + AB_SEENA) = 1u;
    } else lds_wait((LDSP unsigned*)((lds_ptr)lds + AB_SEENA), 1);
    STAMP(2, 0);
    {
        const char* kb = ws + WS_K + (size_t)head * 65536 + j * 2048;
        const char* vb = ws + WS_V + (size_t)head * 65536 + j * 2048;
        auto fetch = [&](int p) {
            const int cp = (chunk + p) & 7;
            glds16_dev(lane * 16, kb + cp * 8192, lds0 + AB_K + p * 8192 + j * 2048); glds16_dev(lane * 16, kb + cp * 8192 + 1024, lds0 + AB_K + p * 8192 + j * 2048 + 1024);
            glds16_dev(lane * 16, vb + cp * 8192, lds0 + AB_V + p * 8192 + j * 2048); glds16_dev(lane * 16, vb + cp * 8192 + 1024, lds0 + AB_V + p * 8192 + j * 2048 + 1024);
        };
        LDSP volatile unsigned* land = (LDSP volatile unsigned*)((lds_ptr)lds + AB_LAND) + j;
#define LANDED(N, p) do { WAIT_V(N); if (lane == 0) *land = p; } while (0)
        fetch(1); fetch(2);
        LANDED(4, 1);
        STAMP(2, 1);
        fetch(3); fetch(4); fetch(5); fetch(6); fetch(7);
        LANDED(20, 2); LANDED(16, 3); LANDED(12, 4); LANDED(8, 5); LANDED(4, 6); LANDED(0, 7);
#undef LANDED
        STAMP(2, 2);
    }
    const int ce = blockIdx.x & 7, tp = 4 * ce + ((blockIdx.x >> 3) & 3), ct = blockIdx.x >> 5;
    const char* wb3 = (const char*)wo + (size_t)((j & 1) * 32) * 1024 + ct * 128 + (j >> 1) * 64;
    const unsigned wvo3 = (lane >> 2) * 1024 + (lane & 3) * 16;
    {
        u32x4 d0, d1, d2, d3;
        const unsigned pv = (128 * ((blockIdx.x >> 3) & 3) + 32 * j + (lane >> 3)) * 1024 + ct * 128 + (lane & 7) * 16;
        asm volatile("global_load_dwordx4 %0, %4, %8\n\tglobal_load_dwordx4 %1, %5, %8\n\tglobal_load_dwordx4 %2, %6, %8\n\tglobal_load_dwordx4 %3, %7, %8\n\ts_waitcnt vmcnt(0)"
                     : "=&v"(d0), "=&v"(d1), "=&v"(d2), "=&v"(d3) : "v"(pv), "v"(pv + 8192), "v"(pv + 16384), "v"(pv + 24576), "s"(wo) : "memory");
    }
    if (blockIdx.x < 86) ffn_unit(lds, x, wg, wu, wd, ws, blockIdx.x, j, lane, tag);
    else if (blockIdx.x < 94) ffn_reduce(lds, ws, blockIdx.x - 86, j, lane, tag);
    STAMP(2, 3);
    if (blockIdx.x == 255 && j == 0) {
#pragma unroll
        for (int i = 0; i < 4; ++i) wait_flags(ws + WS_FLAG_A + (64 * i + lane) * 128, true, tag);
        if (lane == 0) st_dev(ws + WS_EPOCH, tag);
    }
    lds_wait_slow((LDSP unsigned*)((lds_ptr)lds + AB_KVDONE), 8);
#pragma unroll
    for (int s = 0; s < 8; ++s)
#pragma unroll
        for (int q = 0; q < 2; ++q) glds16(wvo3, wb3 + (size_t)(64 * s + 16 * q) * 1024, lds0 + s * 16384 + 8192 + (2 * j + q) * 1024);
    if (j == 0) {
        lds_wait((LDSP unsigned*)((lds_ptr)lds + AB_ODONE), 8);
        if (lane == 0) st_dev(ws + WS_FLAG_O + (head * 8 + chunk) * 128, tag);
        wait_flags(lane < 32 ? ws + WS_FLAG_O + (lane * 8 + ce) * 128 : ws + WS_FLAG_Y + ct * 128, lane <= 32, tag);
        if (lane == 0) *(LDSP volatile unsigned*)((lds_ptr)lds + AB_SEEN) = 1u;
    } else lds_wait((LDSP unsigned*)((lds_ptr)lds + AB_SEEN), 1);
    STAMP(2, 4);
    {
        const char* ob = ws + WS_O + (size_t)(2 * tp + (j >> 1)) * 32768 + (j & 1) * 2048;
#pragma unroll
        for (int s = 0; s < 8; ++s)
#pragma unroll
            for (int q = 0; q < 2; ++q) glds16_dev(lane * 16, ob + s * 4096 + q * 1024, lds0 + s * 16384 + (2 * j + q) * 1024);
        LDSP volatile unsigned* land = (LDSP volatile unsigned*)((lds_ptr)lds + AB_LANDC) + j;
#define LANDED(N, p) do { WAIT_V(N); if (lane == 0) *land = p; } while (0)
        LANDED(12, 2); LANDED(8, 4); LANDED(4, 6); LANDED(0, 8);
#undef LANDED
    }
    STAMP(2, 5);
}

__global__ __launch_bounds__(768) void kab(const u16* __restrict__ x, const u16* __restrict__ wqkv, const u16* __restrict__ w_r, const u16* __restrict__ gate_w,
                                          const u16* __restrict__ wg, const u16* __restrict__ wu, const u16* __restrict__ wd, const u16* __restrict__ wo,
                                          char* __restrict__ ws, u16* __restrict__ out) {
    __shared__ __attribute__((aligned(16))) char lds[AB_LDS];
    const int tid = threadIdx.x, lane = tid & 63, w = __builtin_amdgcn_readfirstlane(tid >> 6);
    const int b = blockIdx.x, xcd = b & 7, loc = b >> 3;
    const int head = xcd * 4 + (loc >> 3), chunk = loc & 7;
    if (w >= 8) { ab_loader(lds, x, wqkv, w_r, gate_w, wg, wu, wd, wo, ws, head, chunk, w - 8, lane); return; }
    const int r = lane & 31, h = lane >> 5;
    lds_cptr L = (lds_cptr)lds;
    const int rot = (head + chunk) & 7;
    unsigned xo[4];
#pragma unroll
    for (int ks = 0; ks < 4; ++ks) xo[ks] = (32 * w + r) * 128 + ((2 * ks + h) ^ ((r >> 1) & 7)) * 16;
    const int blk = (lane >> 4) & 1, q4 = (lane & 15) >> 2, p4 = lane & 3;
    const unsigned wq = AB_W + (8 * h + q4) * 64 + blk * 32 + (((p4 & 1) << 1) | (p4 >> 1)) * 8;
    const unsigned wva = blk ? (8 * h + q4) * 8 : (8 * h + q4) * 32 + p4 * 8, wvk = blk ? 128 : 512;
    f32x16 d1 = {}, d2 = {};
    bf16x8 xa[2], qa[2], va[2], xb2[2], qb[2], vb2[2];
    auto rd = [&](int i, int k0, bf16x8 (&xf)[2], bf16x8 (&fq)[2], bf16x8 (&fv)[2]) {
        const int s = (rot + i) & 7;
        lds_cptr sl = L + (i & 3) * AB_SLOT;
        lds_cptr vv = L + wva + (blk ? AB_AUX + s * 512 : (i & 3) * AB_SLOT + AB_W + 4096);
#pragma unroll
        for (int e = 0; e < 2; ++e) {
            const int ks = k0 + e;
            xf[e] = lds_rd128(sl + xo[ks]);
            fq[e] = lds_tr2(sl + wq + ks * 1024);
            s16x4 a = lds_tr(vv + ks * wvk), c = lds_tr(vv + ks * wvk + (wvk >> 2));
            s16x8 t = {a[0], a[1], a[2], a[3], c[0], c[1], c[2], c[3]};
            fv[e] = __builtin_bit_cast(bf16x8, t);
        }
        SBAR();
    };
    auto mm = [&](bf16x8 (&xf)[2], bf16x8 (&fq)[2], bf16x8 (&fv)[2]) {
#pragma unroll
        for (int e = 0; e < 2; ++e) { d1 = MFMA32(fq[e], xf[e], d1); d2 = MFMA32(xf[e], fv[e], d2); }
        SBAR();
    };
#define LBAR() asm volatile("s_waitcnt lgkmcnt(0)\n\ts_barrier" ::: "memory")
    *(LDSP unsigned*)((lds_ptr)lds + AB_AUX + tid * 8) = (unsigned)w_r[tid] | ((unsigned)gate_w[tid] << 16);
    LBAR(); STAMP(0, 0);
    rd(0, 0, xa, qa, va);
#define A_STAGE(i) rd(i, 2, xb2, qb, vb2); mm(xa, qa, va); if ((i) < 7) { LBAR(); rd((i) + 1, 0, xa, qa, va); } mm(xb2, qb, vb2);
    A_STAGE(0) A_STAGE(1) A_STAGE(2) A_STAGE(3) A_STAGE(4) A_STAGE(5) A_STAGE(6) A_STAGE(7)
#undef A_STAGE
    STAMP(0, 1);
    u32x4 qw, kw, vw0, vw1;
#pragma unroll
    for (int i = 0; i < 4; ++i) {
        kw[i] = cvtpk(d1[8 + 2 * i], d1[8 + 2 * i + 1]);
        vw0[i] = cvtpk(d2[2 * i], d2[2 * i + 1]); vw1[i] = cvtpk(d2[8 + 2 * i], d2[8 + 2 * i + 1]);
    }
    const bool isv = r < 16;
    const unsigned vpos = h * 256 + swap23(r & 15) * 16;
    {
        char* kg = ws + WS_K + (size_t)head * 65536 + (8 * chunk + w) * 1024;
        char* vg = ws + WS_V + (size_t)head * 65536 + (8 * chunk + w) * 1024;
        st16_dev(lane * 16, kw, kg);
        if (isv) { st16_dev(vpos, vw0, vg); st16_dev(vpos + 512, vw1, vg); }
    }
#pragma unroll
    for (int i = 0; i < 4; ++i) qw[i] = cvtpk(d1[2 * i] * QSCALE, d1[2 * i + 1] * QSCALE);
    const bf16x8 qf = __builtin_bit_cast(bf16x8, qw);
    STAMP(0, 2);
    LBAR();
    STAMP(0, 3);
    *(LDSP u32x4*)((lds_ptr)lds + AB_K + w * 1024 + lane * 16) = kw;
    if (isv) { *(LDSP u32x4*)((lds_ptr)lds + AB_V + w * 1024 + vpos) = vw0; *(LDSP u32x4*)((lds_ptr)lds + AB_V + w * 1024 + vpos + 512) = vw1; }
    { u32x4 ones = {0x3F803F80u, 0x3F803F80u, 0x3F803F80u, 0x3F803F80u}; if (tid < 352) *(LDSP u32x4*)((lds_ptr)lds + AB_ONES + tid * 16) = ones; }
    const int tp = 4 * xcd + (loc & 3), ct = loc >> 2;
    const int tt = w & 1, cg = (w >> 1) & 1, kh = w >> 2;
    const int token = 64 * tp + 32 * tt + r, col = 64 * ct + 32 * cg + 16 * kh + 8 * h;
    const u32x4 xv = *(const u32x4*)(x + token * DM + col);
    LBAR();
    STAMP(0, 4);
    if (w == head) {
        LDSP float* rg = (LDSP float*)((lds_ptr)lds + AB_RG);
        if ((r & 30) == 16) {
#pragma unroll
            for (int qd = 0; qd < 4; ++qd) *(LDSP f32x4*)(rg + (r & 1) * 32 + 8 * qd + 4 * h) = f32x4{d2[4 * qd], d2[4 * qd + 1], d2[4 * qd + 2], d2[4 * qd + 3]};
        }
        const int token = 256 * chunk + 32 * w + r;
        const unsigned rb = cvtpk(rg[r], 0.f) & 0xFFFFu;
        unsigned key = (((rb & 0x8000u) ? (~rb & 0xFFFFu) : (rb | 0x8000u)) << 16) | (0xFFFFu - token);
#pragma unroll
        for (int o = 16; o >= 1; o >>= 1) { const unsigned t = __shfl_xor(key, o); key = t > key ? t : key; }
        if (lane == 0) st_dev(ws + WS_KEYS + (8 * chunk + w) * 8, (*(LDSP const u64*)((lds_ptr)lds + AB_TAG) << 32) | key);
        const float gs = rbf(rg[32 + r]);
        const float g = rbf(1.f / (1.f + __expf(-gs)));
        if (lane < 32) __hip_atomic_store((unsigned*)(ws + WS_GATE) + token, __float_as_uint(g), __ATOMIC_RELAXED, __HIP_MEMORY_SCOPE_AGENT);
    }
    lds_cptr kp = L + AB_K + lane * 16;
    lds_cptr vp = isv ? L + AB_V + h * 256 + r * 16 : L + AB_ONES + ((r >= 20 && r < 28) ? 64 : 0);
    const unsigned vinc = isv ? 4096u : 0u;
    f32x16 zero = {};
    bf16x8 kf = lds_rd128(kp);
    f32x16 s0 = MFMA32(kf, qf, zero);
    const float mref = max16(s0);
    f32x16 cneg;
#pragma unroll
    for (int i = 0; i < 16; ++i) { cneg[i] = -mref; s0[i] -= mref; }
    f32x16 o = {}, s1;
    kf = lds_rd128(kp + 1024);
    bf16x8 v0 = lds_rd128(vp), v1 = lds_rd128(vp + 512);
    u32x4 pa, pb = {0u, 0u, 0u, 0u};
#define EX(S, i) S[i] = __builtin_amdgcn_exp2f(S[i]); SBAR();
#define CV(P, S, i) P[i & 3] = cvtpk(S[2 * (i)], S[2 * (i) + 1]); SBAR();
#define STEP(SC, SN, u) \
        SN = MFMA32(kf, qf, cneg); SBAR(); \
        kf = lds_rd128(kq + ((u) + 2) * 1024); SBAR(); \
        EX(SC, 0) EX(SC, 1) EX(SC, 2) EX(SC, 3) \
        o = MFMA32(v1, __builtin_bit_cast(bf16x8, pb), o); SBAR(); \
        v1 = lds_rd128(vp + (u) * 1024 + 512); SBAR(); \
        CV(pa, SC, 0) EX(SC, 4) EX(SC, 5) CV(pa, SC, 1) EX(SC, 6) EX(SC, 7) CV(pa, SC, 2) EX(SC, 8) EX(SC, 9) CV(pa, SC, 3) EX(SC, 10) EX(SC, 11) \
        o = MFMA32(v0, __builtin_bit_cast(bf16x8, pa), o); SBAR(); \
        v0 = lds_rd128(vp + ((u) + 1) * 1024); SBAR(); \
        CV(pb, SC, 4) EX(SC, 12) EX(SC, 13) CV(pb, SC, 5) EX(SC, 14) EX(SC, 15) CV(pb, SC, 6) CV(pb, SC, 7)
    lds_cptr kq = kp;
    auto wait_words = [&](int off, unsigned p) -> unsigned {
        unsigned have = 0;
#pragma unroll 1
        for (unsigned spin = 0; spin < (1u << 20); ++spin) {
            const u32x4 v = *(LDSP volatile const u32x4*)((lds_ptr)lds + off);
            have = __builtin_amdgcn_readfirstlane(min(min(v[0], v[1]), min(v[2], v[3])));
            if (have >= p) break;
            __builtin_amdgcn_s_sleep(1);
        }
        asm volatile("" ::: "memory");
        return have;
    };
    unsigned kv_have = 0;
    auto landed = [&](unsigned p) { if (kv_have < p) kv_have = wait_words(AB_LAND, p); };
    lds_count((LDSP unsigned*)((lds_ptr)lds + AB_ADONE));
    STAMP(0, 5);
#define PRIO_MODE 1
#define PRIO_K 5
    if (w < 4) __builtin_amdgcn_s_setprio(0); else __builtin_amdgcn_s_setprio(1);
    unsigned special = 0x2AAC | (w < 4 ? 1u << PRIO_K : 0u);
#pragma unroll 1
    for (int m = 0; m < 16; ++m) {
        if (__builtin_expect((special >> m) & 1, 0)) {
            if (m == PRIO_K && w < 4) __builtin_amdgcn_s_setprio(2);
            if (m == 2) {
                STAMP(1, 0);
                landed(1);
                STAMP(1, 1);
                s0 = MFMA32(lds_rd128(kq), qf, cneg); kf = lds_rd128(kq + 1024); v0 = lds_rd128(vp);
            } else if (m & 1) landed((m + 1) >> 1);
            if (kv_have >= 7) special &= (1u << PRIO_K);
        }
        SBAR();
        STEP(s0, s1, 0) STEP(s1, s0, 1) STEP(s0, s1, 2) STEP(s1, s0, 3)
        kq += 4096; vp += vinc;
    }
#undef STEP
#undef EX
#undef CV
    o = MFMA32(v1, __builtin_bit_cast(bf16x8, pb), o);
    STAMP(1, 2);
    float l = o[8];
    float chk = 0.f;
#pragma unroll
    for (int i = 0; i < 8; ++i) chk += fabsf(o[i]);
    const bool bad = !(l < 1.152921504606847e18f) || !(chk < 3.0e38f);
    if (__any(bad)) {
        float m = -INFINITY;
        o = f32x16{};
        lds_cptr vq = isv ? L + AB_V + h * 256 + r * 16 : L + AB_ONES;
        for (int t = 0; t < 64; ++t) {
            f32x16 s = MFMA32(lds_rd128(kp + t * 1024), qf, zero);
            const float mn = fmaxf(m, max16(s));
            const float f = __builtin_amdgcn_exp2f(m - mn);
            m = mn;
#pragma unroll
            for (int i = 0; i < 16; ++i) { s[i] -= mn; o[i] *= f; }
            bf16x8 p0, p1;
            exp_pack(s, p0, p1);
            lds_cptr vt = vq + (isv ? t * 1024 : 0);
            o = MFMA32(lds_rd128(vt), p0, o); o = MFMA32(lds_rd128(vt + 512), p1, o);
        }
        l = o[8];
    }
    asm volatile("s_waitcnt lgkmcnt(0)" ::: "memory");
    if (lane == 0) __hip_atomic_fetch_add((LDSP unsigned*)((lds_ptr)lds + AB_KVDONE), 1u, __ATOMIC_RELAXED, __HIP_MEMORY_SCOPE_WORKGROUP);
    const float inv = 1.f / l;
    u32x4 ov;
#pragma unroll
    for (int i = 0; i < 4; ++i) ov[i] = cvtpk(o[2 * i] * inv, o[2 * i + 1] * inv);
    st16_dev(lane * 16, ov, ws + WS_O + (size_t)((8 * chunk + w) * 32 + head) * 1024);
    __builtin_amdgcn_s_setprio(0);
    lds_count((LDSP unsigned*)((lds_ptr)lds + AB_ODONE));
    STAMP(1, 3);
    const unsigned wq3 = 8192 + cg * 4096 + (8 * h + q4) * 64 + blk * 32 + (((p4 & 1) << 1) | (p4 >> 1)) * 8 + kh * 2048;
    const unsigned oo3 = tt * 4096 + kh * 2048 + lane * 16;
    f32x16 acc = {};
    u32x4 y0, y1; unsigned gtb, idxb;
    bf16x8 oa[4], wa[4], ob4[4], wb4[4];
    auto c_rd = [&](int g2, bf16x8 (&of)[4], bf16x8 (&wf)[4]) {
#pragma unroll
        for (int i = 0; i < 4; ++i) { lds_cptr sb = L + (2 * g2 + (i >> 1)) * 16384 + (i & 1) * 1024; of[i] = lds_rd128(sb + oo3); wf[i] = lds_tr2(sb + wq3); }
        SBAR();
    };
    auto c_mm = [&](bf16x8 (&of)[4], bf16x8 (&wf)[4]) {
#pragma unroll
        for (int i = 0; i < 4; ++i) acc = MFMA32(wf[i], of[i], acc);
        SBAR();
    };
    lds_wait((LDSP unsigned*)((lds_ptr)lds + AB_SEEN), 1);
    asm volatile("global_load_dwordx4 %0, %4, %5 sc1\n\tglobal_load_dwordx4 %1, %4, %5 offset:16 sc1\n\tglobal_load_dword %2, %6, %7 sc1\n\tglobal_load_dword %3, %8, %5 offset:2048 sc1"
                 : "=&v"(y0), "=&v"(y1), "=&v"(gtb), "=&v"(idxb) : "v"(col * 4), "s"(ws + WS_Y2), "v"(token * 4), "s"(ws + WS_GATE), "v"(ct * 4) : "memory");
    unsigned c_have = wait_words(AB_LANDC, 2);
    STAMP(1, 4);
    c_rd(0, oa, wa);
    if (c_have < 4) c_have = wait_words(AB_LANDC, 4);
    c_rd(1, ob4, wb4); c_mm(oa, wa);
    if (c_have < 6) c_have = wait_words(AB_LANDC, 6);
    c_rd(2, oa, wa); c_mm(ob4, wb4);
    if (c_have < 8) c_have = wait_words(AB_LANDC, 8);
    c_rd(3, ob4, wb4); c_mm(oa, wa); c_mm(ob4, wb4);
    STAMP(4, 0);
    {
        LDSP f32x4* xw = (LDSP f32x4*)((lds_ptr)lds + AB_XCH + w * 2048) + lane;
        xw[0] = kh ? f32x4{acc[0], acc[1], acc[2], acc[3]} : f32x4{acc[8], acc[9], acc[10], acc[11]};
        xw[64] = kh ? f32x4{acc[4], acc[5], acc[6], acc[7]} : f32x4{acc[12], acc[13], acc[14], acc[15]};
    }
    STAMP(4, 1);
    asm volatile("s_waitcnt vmcnt(0) lgkmcnt(0)\n\ts_barrier" : "+v"(y0), "+v"(y1), "+v"(gtb), "+v"(idxb)::"memory");
    STAMP(4, 2);
    {
        LDSP const f32x4* xr = (LDSP const f32x4*)((lds_ptr)lds + AB_XCH + (w ^ 4) * 2048) + lane;
        const f32x4 pa0 = xr[0], pa1 = xr[64];
        const bool sel = token == (int)idxb;
        const float gt = __uint_as_float(gtb), omg = rbf(1.f - gt);
        u32x4 res;
#pragma unroll
        for (int i = 0; i < 4; ++i) {
            float o2[2];
#pragma unroll
            for (int e = 0; e < 2; ++e) {
                const int c = 2 * i + e;
                const float a = rbf((kh ? acc[8 + c] : acc[c]) + (c < 4 ? pa0[c & 3] : pa1[c & 3]));
                const float xx = e ? __uint_as_float(xv[i] & 0xFFFF0000u) : __uint_as_float(xv[i] << 16);
                const float ys = __uint_as_float(c < 4 ? y0[c & 3] : y1[c & 3]);
                const float bb = rbf(xx + (sel ? ys : 0.f));
                o2[e] = rbf(gt * a) + rbf(omg * bb);
            }
            res[i] = cvtpk(o2[0], o2[1]);
        }
        STAMP(4, 3);
        *(u32x4*)(out + token * DM + col) = res;
    }
    STAMP(1, 5);
}

extern "C" void kernel_launch(void* const* d_in, const int* in_sizes, int n_in, void* d_out, int out_size, void* d_ws, size_t ws_size, hipStream_t stream) {
    const u16* x = (const u16*)d_in[0];
    const u16* wqkv = (const u16*)d_in[1];
    const u16* wo = (const u16*)d_in[2];
    const u16* w_r = (const u16*)d_in[4];
    const u16* wg = (const u16*)d_in[5];
    const u16* wu = (const u16*)d_in[6];
    const u16* wd = (const u16*)d_in[7];
    const u16* gate_w = (const u16*)d_in[8];
    char* ws = (char*)d_ws;
    kab<<<256, 768, 0, stream>>>(x, wqkv, w_r, gate_w, wg, wu, wd, wo, ws, (u16*)d_out);
}
```

```cpp
#include <hip/hip_runtime.h>
#include <hip/hip_bf16.h>
#include <stdint.h>

typedef __bf16 bf16x8 __attribute__((ext_vector_type(8)));
typedef __bf16 bf16x2 __attribute__((ext_vector_type(2)));
typedef short s16x4 __attribute__((ext_vector_type(4)));
typedef short s16x8 __attribute__((ext_vector_type(8)));
typedef float f32x16 __attribute__((ext_vector_type(16)));
typedef float f32x4 __attribute__((ext_vector_type(4)));
typedef float f32x2 __attribute__((ext_vector_type(2)));
typedef unsigned u32x4 __attribute__((ext_vector_type(4)));
typedef unsigned short u16;
#define LDSP __attribute__((address_space(3)))
typedef LDSP const char* lds_cptr;
typedef LDSP char* lds_ptr;
#define DI __device__ __forceinline__
#define MFMA32(a, b, c) __builtin_amdgcn_mfma_f32_32x32x16_bf16(a, b, c, 0, 0, 0)
#define SBAR() __builtin_amdgcn_sched_barrier(0)

#define MAPPING 0
#define STAMP(kid, i)
constexpr int SEQ = 2048, DM = 512, NH = 32, FF = 1376;
constexpr float QSCALE = 0.25f * 1.4426950408889634f;

constexpr size_t WS_Q = 0, WS_K = 2u << 20, WS_V = 4u << 20, WS_O = 6u << 20;
constexpr size_t WS_GATE = 8u << 20;
constexpr size_t WS_KEYS = WS_GATE + 8192;
constexpr size_t WS_H = WS_KEYS + 1024;
constexpr size_t WS_YP = WS_H + 8192;
constexpr size_t WS_EPOCH = 9u << 20;
constexpr size_t WS_FLAG_F = WS_EPOCH + 4096;
constexpr size_t WS_Y2 = WS_FLAG_F + 16384;
constexpr size_t WS_FLAG_Y = WS_Y2 + 4096;
constexpr size_t WS_FLAG_A = 10u << 20;
constexpr size_t WS_FLAG_O = WS_FLAG_A + 131072;

DI unsigned cvtpk(float lo, float hi) { f32x2 v = {lo, hi}; bf16x2 b = __builtin_convertvector(v, bf16x2); return __builtin_bit_cast(unsigned, b); }
DI float bf2f(unsigned b) { return __uint_as_float(b << 16); }
DI float rbf(float x) { return __uint_as_float(cvtpk(x, 0.f) << 16); }
DI unsigned lds_addr(const void* p) { return (unsigned)(size_t)(LDSP const char*)p; }
DI void glds16(unsigned voff, const void* sbase, unsigned ldsdst) {
    asm volatile("s_mov_b32 m0, %2\n\ts_nop 0\n\tglobal_load_lds_dwordx4 %0, %1" ::"v"(voff), "s"(sbase), "s"(ldsdst) : "memory");
}
DI bf16x8 lds_rd128(lds_cptr p) { return *(LDSP const bf16x8*)p; }
DI s16x4 lds_tr(lds_cptr p) { return __builtin_bit_cast(s16x4, __builtin_amdgcn_ds_read_tr16_b64_v4i16((LDSP s16x4*)p)); }
DI bf16x8 lds_tr2(lds_cptr p) {
    s16x4 a = lds_tr(p), b = lds_tr(p + 256);
    s16x8 r = {a[0], a[1], a[2], a[3], b[0], b[1], b[2], b[3]};
    return __builtin_bit_cast(bf16x8, r);
}
#define WAIT_BAR(N) asm volatile("s_waitcnt vmcnt(" #N ") lgkmcnt(0)\n\ts_barrier" ::: "memory")
DI int swap23(int v) { return (v & ~12) | ((v & 4) << 1) | ((v & 8) >> 1); }

constexpr int AB_SLOT = 39168, AB_W = 32768, AB_AUX = 4 * AB_SLOT;
constexpr int AB_K = 0, AB_V = 65536, AB_ONES = 131072;
constexpr int AB_FS = AB_ONES + 5632, AB_FH = AB_FS + 1024, AB_FC = AB_FH + 64;
constexpr int AB_XCH_ = AB_FS + 1536;
constexpr int AB_LAND = AB_FC + 16;
constexpr int AB_PROG = AB_LAND + 16;
constexpr int AB_LANDS = AB_FC + 64, AB_LANDW = AB_FC + 96;
constexpr int AB_KVDONE = AB_FC + 112, AB_SEEN = AB_FC + 116, AB_TAG = AB_FC + 128;
constexpr int AB_RG = AB_XCH_ + 16384;
constexpr int AB_XCH = AB_XCH_;
constexpr int AB_FW = AB_FS + 1280;
constexpr int AB_LDS = AB_AUX + 4096;
static_assert(AB_LDS <= 163840 && AB_FW + 16384 <= AB_LDS && AB_RG + 256 <= AB_AUX && AB_FW % 256 == 0, "LDS");

DI float max16(const f32x16& s) {
    float a = fmaxf(fmaxf(s[0], s[1]), s[2]), b = fmaxf(fmaxf(s[3], s[4]), s[5]);
    a = fmaxf(fmaxf(a, s[6]), s[7]); b = fmaxf(fmaxf(b, s[8]), s[9]);
    a = fmaxf(fmaxf(a, s[10]), s[11]); b = fmaxf(fmaxf(b, s[12]), s[13]);
    a = fmaxf(fmaxf(a, s[14]), s[15]);
    float m = fmaxf(a, b);
    auto rr = __builtin_amdgcn_permlane32_swap(__float_as_uint(m), __float_as_uint(m), false, false);
    return fmaxf(__uint_as_float(rr[0]), __uint_as_float(rr[1]));
}
DI void exp_pack(const f32x16& s, bf16x8& p0, bf16x8& p1) {
    u32x4 a, b;
#pragma unroll
    for (int i = 0; i < 4; ++i) a[i] = cvtpk(__builtin_amdgcn_exp2f(s[2 * i]), __builtin_amdgcn_exp2f(s[2 * i + 1]));
#pragma unroll
    for (int i = 0; i < 4; ++i) b[i] = cvtpk(__builtin_amdgcn_exp2f(s[8 + 2 * i]), __builtin_amdgcn_exp2f(s[8 + 2 * i + 1]));
    p0 = __builtin_bit_cast(bf16x8, a); p1 = __builtin_bit_cast(bf16x8, b);
}
DI void st16_dev(unsigned voff, u32x4 d, void* sbase) { asm volatile("global_store_dwordx4 %0, %1, %2 sc1\n\ts_nop 1" ::"v"(voff), "v"(d), "s"(sbase) : "memory"); }
DI void glds16_dev(unsigned voff, const void* sbase, unsigned ldsdst) {
    asm volatile("s_mov_b32 m0, %2\n\ts_nop 0\n\tglobal_load_lds_dwordx4 %0, %1 sc1" ::"v"(voff), "s"(sbase), "s"(ldsdst) : "memory");
}
typedef unsigned long long u64;
DI u64 ld_dev(const void* p) { return __hip_atomic_load((const u64*)p, __ATOMIC_RELAXED, __HIP_MEMORY_SCOPE_AGENT); }
DI void st_dev(void* p, u64 v) { __hip_atomic_store((u64*)p, v, __ATOMIC_RELAXED, __HIP_MEMORY_SCOPE_AGENT); }
DI void wait_flags(const char* flag, bool want, u64 tag) {
#pragma unroll 1
    for (unsigned spin = 0; spin < (1u << 20); ++spin) {
        const u64 v = want ? ld_dev(flag) : tag;
        if (__all(v == tag)) break;
        __builtin_amdgcn_s_sleep(1);
    }
}

#define BARRIER() asm volatile("s_barrier" ::: "memory")
#define WAIT_V(N) asm volatile("s_waitcnt vmcnt(" #N ")" ::: "memory")

DI void wait_flags_slow(const char* flag, bool want, u64 tag) {
#pragma unroll 1
    for (unsigned spin = 0; spin < (1u << 18); ++spin) {
        const u64 v = want ? ld_dev(flag) : tag;
        if (__all(v == tag)) break;
        __builtin_amdgcn_s_sleep(16);
    }
}
DI f32x4 mfma16(bf16x8 a, bf16x8 b, f32x4 c) { return __builtin_amdgcn_mfma_f32_16x16x32_bf16(a, b, c, 0, 0, 0); }
DI void lds_count(LDSP unsigned* c) { asm volatile("s_waitcnt vmcnt(0) lgkmcnt(0)" ::: "memory"); if ((threadIdx.x & 63) == 0) __hip_atomic_fetch_add(c, 1u, __ATOMIC_RELAXED, __HIP_MEMORY_SCOPE_WORKGROUP); }
DI void lds_wait(LDSP unsigned* c, unsigned n) {
#pragma unroll 1
    for (unsigned spin = 0; spin < (1u << 20); ++spin) {
        if (__hip_atomic_load(c, __ATOMIC_RELAXED, __HIP_MEMORY_SCOPE_WORKGROUP) >= n) break;
        __builtin_amdgcn_s_sleep(1);
    }
    asm volatile("" ::: "memory");
}
DI void lds_wait_idle(LDSP unsigned* c, unsigned n) {
    __builtin_amdgcn_s_setprio(0);
#pragma unroll 1
    for (unsigned spin = 0; spin < (1u << 18); ++spin) {
        if (__hip_atomic_load(c, __ATOMIC_RELAXED, __HIP_MEMORY_SCOPE_WORKGROUP) >= n) break;
        __builtin_amdgcn_s_sleep(32);
    }
    asm volatile("" ::: "memory");
    __builtin_amdgcn_s_setprio(3);
}
DI void wake_all() { asm volatile("s_waitcnt lgkmcnt(0)\n\ts_wakeup" ::: "memory"); }
DI unsigned top_key_dev(char* ws, int lane, u64 tag) {
    u64 v = 0;
#pragma unroll 1
    for (unsigned spin = 0; spin < (1u << 20); ++spin) {
        v = ld_dev(ws + WS_KEYS + lane * 8);
        if (__all((unsigned)(v >> 32) == (unsigned)tag)) break;
        __builtin_amdgcn_s_sleep(8);
    }
    unsigned key = (unsigned)v;
#pragma unroll
    for (int o = 32; o >= 1; o >>= 1) { const unsigned t = __shfl_xor(key, o); key = t > key ? t : key; }
    return __builtin_amdgcn_readfirstlane(key);
}
DI bf16x8 tr_frag(lds_cptr p, int step) { s16x4 a = lds_tr(p), c = lds_tr(p + step); s16x8 t = {a[0], a[1], a[2], a[3], c[0], c[1], c[2], c[3]}; return __builtin_bit_cast(bf16x8, t); }
DI void ffn_unit(char* lds, const u16* x, const u16* wg, const u16* wu, const u16* wd, char* ws, int u, int j, int lane, u64 tag) {
    const int n = lane & 15, g = lane >> 4, q = n >> 2, p = n & 3;
    const unsigned slot = lds_addr(lds) + AB_FW + j * 4096;
    lds_cptr sl = (lds_cptr)lds + AB_FW + j * 4096;
    bf16x8 ag[4], au[4], ad[8];
    const unsigned vo = (lane >> 1) * (FF * 2) + (lane & 1) * 16;
    const size_t wo = (size_t)(128 * j) * (FF * 2) + 32 * u;
#pragma unroll
    for (int i = 0; i < 4; ++i) glds16(vo, (const char*)wg + wo + (size_t)i * 32 * (FF * 2), slot + i * 1024);
    asm volatile("s_waitcnt vmcnt(0)" ::: "memory");
#pragma unroll
    for (int ks = 0; ks < 4; ++ks) ag[ks] = tr_frag(sl + (32 * ks + 8 * g + q) * 32 + p * 8, 128);
    asm volatile("s_waitcnt lgkmcnt(0)" : "+v"(ag[0]), "+v"(ag[1]), "+v"(ag[2]), "+v"(ag[3])::"memory");
#pragma unroll
    for (int i = 0; i < 4; ++i) glds16(vo, (const char*)wu + wo + (size_t)i * 32 * (FF * 2), slot + i * 1024);
    asm volatile("s_waitcnt vmcnt(0)" ::: "memory");
#pragma unroll
    for (int ks = 0; ks < 4; ++ks) au[ks] = tr_frag(sl + (32 * ks + 8 * g + q) * 32 + p * 8, 128);
    asm volatile("s_waitcnt lgkmcnt(0)" : "+v"(au[0]), "+v"(au[1]), "+v"(au[2]), "+v"(au[3])::"memory");
#pragma unroll
    for (int i = 0; i < 4; ++i) glds16((lane >> 4) * 1024 + (lane & 15) * 16, (const char*)wd + (size_t)(16 * u + 4 * i) * 1024 + j * 256, slot + i * 1024);
    const unsigned key = top_key_dev(ws, lane, tag);
    const int idx = 0xFFFF - (key & 0xFFFF);
    bf16x8 xs[4];
#pragma unroll
    for (int ks = 0; ks < 4; ++ks) xs[ks] = *(const bf16x8*)(x + idx * DM + 128 * j + 32 * ks + 8 * g);
    asm volatile("s_waitcnt vmcnt(0)" ::: "memory");
#pragma unroll
    for (int t = 0; t < 8; ++t) ad[t] = tr_frag(sl + (8 * (g & 1) + q) * 256 + t * 32 + p * 8, 1024);
    f32x4 dg = {}, du = {};
#pragma unroll
    for (int ks = 0; ks < 4; ++ks) { dg = mfma16(ag[ks], xs[ks], dg); du = mfma16(au[ks], xs[ks], du); }
    LDSP float* fs = (LDSP float*)((lds_ptr)lds + AB_FS);
    LDSP unsigned* fc = (LDSP unsigned*)((lds_ptr)lds + AB_FC);
    if (n == 0) { *(LDSP f32x4*)(fs + j * 32 + 4 * g) = dg; *(LDSP f32x4*)(fs + j * 32 + 16 + 4 * g) = du; }
    lds_count(fc);
    if (j == 0) {
        lds_wait(fc, 4);
        const int l32 = lane & 31;
        const float v = rbf((fs[l32] + fs[32 + l32]) + (fs[64 + l32] + fs[96 + l32]));
        const float up = __shfl(v, (lane & 15) + 16);
        const float hv = rbf(v / (1.f + __expf(-v))) * up;
        if (lane < 16) *(LDSP u16*)((lds_ptr)lds + AB_FH + lane * 2) = (u16)(cvtpk(hv, 0.f) & 0xFFFFu);
        lds_count(fc + 1);
    }
    lds_wait(fc + 1, 1);
    u32x4 hb = *(LDSP const u32x4*)((lds_ptr)lds + AB_FH + (g & 1) * 16);
    if (g >= 2) hb = u32x4{0u, 0u, 0u, 0u};
    char* yb = ws + WS_YP + (size_t)u * 2048 + j * 512;
    f32x4 y[8];
#pragma unroll
    for (int t = 0; t < 8; ++t) y[t] = mfma16(ad[t], __builtin_bit_cast(bf16x8, hb), f32x4{});
    asm volatile("s_nop 7\n\ts_nop 1" : "+v"(y[0]), "+v"(y[1]), "+v"(y[2]), "+v"(y[3]), "+v"(y[4]), "+v"(y[5]), "+v"(y[6]), "+v"(y[7]));
    if (n == 0) {
#pragma unroll
        for (int t = 0; t < 8; ++t) st16_dev(t * 64 + g * 16, __builtin_bit_cast(u32x4, y[t]), yb);
    }
    lds_count(fc + 2);
    if (j == 0) { lds_wait(fc + 2, 4); if (lane == 0) st_dev(ws + WS_FLAG_F + u * 128, tag); }
}
DI void ffn_reduce(char* lds, char* ws, int g8, int j, int lane, u64 tag) {
    const unsigned key = top_key_dev(ws, lane, tag);
    wait_flags_slow(ws + WS_FLAG_F + lane * 128, true, tag);
    wait_flags_slow(ws + WS_FLAG_F + (64 + lane) * 128, lane < 22, tag);
    const unsigned* yp = (const unsigned*)(ws + WS_YP) + 64 * g8 + lane;
    unsigned v[22];
#pragma unroll
    for (int i = 0; i < 22; ++i) { const int u = min(j + 4 * i, 85); v[i] = __hip_atomic_load(yp + u * DM, __ATOMIC_RELAXED, __HIP_MEMORY_SCOPE_AGENT); }
    float y = 0.f;
#pragma unroll
    for (int i = 0; i < 22; ++i) y += (j + 4 * i < 86) ? __uint_as_float(v[i]) : 0.f;
    LDSP float* fs = (LDSP float*)((lds_ptr)lds + AB_FS);
    LDSP unsigned* fc = (LDSP unsigned*)((lds_ptr)lds + AB_FC);
    fs[j * 64 + lane] = y;
    lds_count(fc);
    if (j == 0) {
        lds_wait(fc, 4);
        const float ys = rbf((fs[lane] + fs[64 + lane]) + (fs[128 + lane] + fs[192 + lane]));
        const unsigned ob16 = key >> 16, rb = (ob16 & 0x8000u) ? (ob16 & 0x7FFFu) : (~ob16 & 0xFFFFu);
        const float rs = rbf(1.f / (1.f + __expf(-bf2f(rb))));
        __hip_atomic_store((unsigned*)(ws + WS_Y2) + 64 * g8 + lane, __float_as_uint(rbf(ys * rs)), __ATOMIC_RELAXED, __HIP_MEMORY_SCOPE_AGENT);
        if (lane == 0) __hip_atomic_store((unsigned*)(ws + WS_Y2) + 512 + g8, 0xFFFFu - (key & 0xFFFFu), __ATOMIC_RELAXED, __HIP_MEMORY_SCOPE_AGENT);
        asm volatile("s_waitcnt vmcnt(0)" ::: "memory");
        if (lane == 0) st_dev(ws + WS_FLAG_Y + g8 * 128, tag);
    }
}

DI void ab_loader(char* lds, const u16* x, const u16* wqkv, const u16* w_r, const u16* gate_w, const u16* wg, const u16* wu, const u16* wd, const u16* wo, char* ws, int head, int chunk, int j, int lane) {
    const unsigned lds0 = lds_addr(lds);
    const int rot = (head + chunk) & 7;
    __builtin_amdgcn_s_setprio(3);
    const unsigned xv0 = (lane >> 3) * 1024 + ((lane & 7) ^ ((lane >> 4) & 7)) * 16;
    const unsigned xv1 = (lane >> 3) * 1024 + ((lane & 7) ^ (((lane >> 4) + 4) & 7)) * 16;
    unsigned wsrc[2];
#pragma unroll
    for (int e = 0; e < 2; ++e) {
        const int o = 768 * (2 * j + e) + 16 * lane;
        if (o < 4096) { const int cc = (o >> 4) & 3; wsrc[e] = (o >> 6) * 3072 + (cc < 2 ? 16 * cc : 1024 + 16 * (cc - 2)); }
        else { const int o2 = o - 4096, row = min(o2 >> 5, 63); wsrc[e] = row * 3072 + 2048 + 16 * ((o2 >> 4) & 1); }
    }
    const char* xb = (const char*)x + (size_t)(256 * chunk + 64 * j) * 1024;
    const char* wb = (const char*)wqkv + head * 32;
    auto issue = [&](int i) {
        const int s = (rot + i) & 7;
        const unsigned sl = lds0 + (i & 3) * AB_SLOT;
        if (lane < 48) { glds16(wsrc[0], wb + (size_t)s * 64 * 3072, sl + AB_W + 768 * (2 * j)); glds16(wsrc[1], wb + (size_t)s * 64 * 3072, sl + AB_W + 768 * (2 * j + 1)); }
#pragma unroll
        for (int q = 0; q < 8; ++q) glds16((q & 1) ? xv1 : xv0, xb + q * 8192 + s * 128, sl + (8 * j + q) * 1024);
    };
    STAMP(3, 1); issue(0); issue(1);
    unsigned e_lo, e_hi;
    asm volatile("global_load_dword %0, %2, %3 sc1\n\tglobal_load_dword %1, %2, %3 offset:4 sc1" : "=&v"(e_lo), "=&v"(e_hi) : "v"(0u), "s"(ws + WS_EPOCH) : "memory");
    WAIT_BAR(12); STAMP(3, 2); issue(2);
    asm volatile("s_waitcnt vmcnt(10)\n\ts_barrier" : "+v"(e_lo), "+v"(e_hi)::"memory");
    const u64 tag = (((u64)__builtin_amdgcn_readfirstlane(e_hi) << 32) | __builtin_amdgcn_readfirstlane(e_lo)) + 1;
    issue(3);
    WAIT_BAR(10); issue(4);
    WAIT_BAR(10); issue(5);
    WAIT_BAR(10); issue(6);
    WAIT_BAR(10); issue(7);
    WAIT_BAR(10);
    WAIT_BAR(0);
    BARRIER();
    if (j == 0) {
        if (lane < 32) *(LDSP unsigned*)((lds_ptr)lds + AB_FC + lane * 4) = 0u;
        if (lane == 0) *(LDSP u64*)((lds_ptr)lds + AB_TAG) = tag;
    }
    asm volatile("s_waitcnt lgkmcnt(0)\n\ts_barrier" ::: "memory");
    wait_flags(ws + WS_FLAG_A + (head * 64 + ((chunk + 1 + (lane >> 1)) & 7) * 8 + 2 * j + (lane & 1)) * 64, lane < 14, tag);
    STAMP(2, 0);
    {
        const char* kb = ws + WS_K + (size_t)head * 65536 + j * 2048;
        const char* vb = ws + WS_V + (size_t)head * 65536 + j * 2048;
        auto fetch = [&](int p) {
            const int cp = (chunk + p) & 7;
            glds16_dev(lane * 16, kb + cp * 8192, lds0 + AB_K + p * 8192 + j * 2048); glds16_dev(lane * 16, kb + cp * 8192 + 1024, lds0 + AB_K + p * 8192 + j * 2048 + 1024);
            glds16_dev(lane * 16, vb + cp * 8192, lds0 + AB_V + p * 8192 + j * 2048); glds16_dev(lane * 16, vb + cp * 8192 + 1024, lds0 + AB_V + p * 8192 + j * 2048 + 1024);
        };
        LDSP volatile unsigned* land = (LDSP volatile unsigned*)((lds_ptr)lds + AB_LAND) + j;
#define LANDED(N, p) do { WAIT_V(N); if (lane == 0) *land = p; } while (0)
        fetch(1); fetch(2);
        LANDED(4, 1);
        STAMP(2, 1);
        fetch(3); fetch(4); fetch(5); fetch(6); fetch(7);
        LANDED(20, 2); LANDED(16, 3); LANDED(12, 4); LANDED(8, 5); LANDED(4, 6); LANDED(0, 7);
#undef LANDED
        STAMP(2, 2);
    }
    __builtin_amdgcn_s_setprio(0);
    const int ce = blockIdx.x & 7, tp = 4 * ce + ((blockIdx.x >> 3) & 3), ct = blockIdx.x >> 5;
    const char* wb3 = (const char*)wo + (size_t)((j & 1) * 32) * 1024 + ct * 128 + (j >> 1) * 64;
    const unsigned wvo3 = (lane >> 2) * 1024 + (lane & 3) * 16;
    {
        u32x4 d0, d1, d2, d3;
        const unsigned pv = (128 * ((blockIdx.x >> 3) & 3) + 32 * j + (lane >> 3)) * 1024 + ct * 128 + (lane & 7) * 16;
        asm volatile("global_load_dwordx4 %0, %4, %8\n\tglobal_load_dwordx4 %1, %5, %8\n\tglobal_load_dwordx4 %2, %6, %8\n\tglobal_load_dwordx4 %3, %7, %8\n\ts_waitcnt vmcnt(0)"
                     : "=&v"(d0), "=&v"(d1), "=&v"(d2), "=&v"(d3) : "v"(pv), "v"(pv + 8192), "v"(pv + 16384), "v"(pv + 24576), "s"(wo) : "memory");
    }
    if (blockIdx.x < 94) {
        __builtin_amdgcn_s_setprio(3);
        if (blockIdx.x < 86) ffn_unit(lds, x, wg, wu, wd, ws, blockIdx.x, j, lane, tag);
        else ffn_reduce(lds, ws, blockIdx.x - 86, j, lane, tag);
        __builtin_amdgcn_s_setprio(0);
    }
    STAMP(2, 3);
    if (blockIdx.x == 255 && j == 1) {
#pragma unroll
        for (int i = 0; i < 4; ++i) wait_flags_slow(ws + WS_FLAG_A + (64 * i + lane) * 512, true, tag);
        if (lane == 0) st_dev(ws + WS_EPOCH, tag);
    }
    if (j == 0) {
        const int T = 2 * tp + (lane & 1);
        wait_flags_slow(lane < 2 ? ws + WS_FLAG_A + ((T & 7) * 64 + T) * 64 : ws + WS_FLAG_Y + ct * 128, lane <= 2, tag);
        if (lane == 0) *(LDSP volatile unsigned*)((lds_ptr)lds + AB_SEEN) = 1u;
    }
    lds_wait_idle((LDSP unsigned*)((lds_ptr)lds + AB_KVDONE), 8);
    STAMP(2, 4);
#pragma unroll
    for (int s = 0; s < 8; ++s)
#pragma unroll
        for (int q = 0; q < 2; ++q) glds16(wvo3, wb3 + (size_t)(64 * s + 16 * q) * 1024, lds0 + s * 16384 + 8192 + (2 * j + q) * 1024);
    {
        const char* fl = ws + WS_FLAG_O + ((4 * (j + 4 * ((lane >> 3) & 1)) + ((lane >> 1) & 3)) * 64 + 2 * tp + (lane & 1)) * 64;
        unsigned todo = 3;
        bool first = true;
#pragma unroll 1
        for (unsigned spin = 0; spin < (1u << 20) && todo; ++spin) {
            const u64 v = ld_dev(fl);
            const unsigned long long ok = __ballot(v == tag);
            if (first) { first = false; if (lane == 0) *((LDSP volatile unsigned*)((lds_ptr)lds + AB_LANDW) + j) = 1u; }
            const unsigned rdy = (((ok & 0xFFull) == 0xFFull ? 1u : 0u) | ((ok & 0xFF00ull) == 0xFF00ull ? 2u : 0u)) & todo;
            if (rdy) {
#pragma unroll
                for (int e = 0; e < 2; ++e) if (rdy & (1u << e)) {
                    const int s = j + 4 * e;
                    const char* ob = ws + WS_O + (size_t)(2 * tp) * 32768 + s * 4096;
#pragma unroll
                    for (int q = 0; q < 8; ++q) glds16_dev(lane * 16, ob + (q >> 2) * 32768 + (q & 3) * 1024, lds0 + s * 16384 + q * 1024);
                }
                WAIT_V(0);
                if (lane < 2 && (rdy & (1u << lane))) *((LDSP volatile unsigned*)((lds_ptr)lds + AB_LANDS) + j + 4 * lane) = 1u;
                todo &= ~rdy;
            } else __builtin_amdgcn_s_sleep(1);
        }
    }
    STAMP(2, 5);
}

__global__ __launch_bounds__(768) void kab(const u16* __restrict__ x, const u16* __restrict__ wqkv, const u16* __restrict__ w_r, const u16* __restrict__ gate_w,
                                          const u16* __restrict__ wg, const u16* __restrict__ wu, const u16* __restrict__ wd, const u16* __restrict__ wo,
                                          char* __restrict__ ws, u16* __restrict__ out) {
    __shared__ __attribute__((aligned(16))) char lds[AB_LDS];
    const int tid = threadIdx.x, lane = tid & 63, w = __builtin_amdgcn_readfirstlane(tid >> 6);
    const int b = blockIdx.x, xcd = b & 7, loc = b >> 3;
    const int head = xcd * 4 + (loc >> 3), chunk = loc & 7;
    if (w >= 8) { ab_loader(lds, x, wqkv, w_r, gate_w, wg, wu, wd, wo, ws, head, chunk, w - 8, lane); return; }
    const int r = lane & 31, h = lane >> 5;
    lds_cptr L = (lds_cptr)lds;
    const int rot = (head + chunk) & 7;
    unsigned xo[4];
#pragma unroll
    for (int ks = 0; ks < 4; ++ks) xo[ks] = (32 * w + r) * 128 + ((2 * ks + h) ^ ((r >> 1) & 7)) * 16;
    const int blk = (lane >> 4) & 1, q4 = (lane & 15) >> 2, p4 = lane & 3;
    const unsigned wq = AB_W + (8 * h + q4) * 64 + blk * 32 + (((p4 & 1) << 1) | (p4 >> 1)) * 8;
    const unsigned wva = blk ? (8 * h + q4) * 8 : (8 * h + q4) * 32 + p4 * 8, wvk = blk ? 128 : 512;
    f32x16 d1 = {}, d2 = {};
    bf16x8 xa[2], qa[2], va[2], xb2[2], qb[2], vb2[2];
    auto rd = [&](int i, int k0, bf16x8 (&xf)[2], bf16x8 (&fq)[2], bf16x8 (&fv)[2]) {
        const int s = (rot + i) & 7;
        lds_cptr sl = L + (i & 3) * AB_SLOT;
        lds_cptr vv = L + wva + (blk ? AB_AUX + s * 512 : (i & 3) * AB_SLOT + AB_W + 4096);
#pragma unroll
        for (int e = 0; e < 2; ++e) {
            const int ks = k0 + e;
            xf[e] = lds_rd128(sl + xo[ks]);
            fq[e] = lds_tr2(sl + wq + ks * 1024);
            s16x4 a = lds_tr(vv + ks * wvk), c = lds_tr(vv + ks * wvk + (wvk >> 2));
            s16x8 t = {a[0], a[1], a[2], a[3], c[0], c[1], c[2], c[3]};
            fv[e] = __builtin_bit_cast(bf16x8, t);
        }
        SBAR();
    };
    auto mm = [&](bf16x8 (&xf)[2], bf16x8 (&fq)[2], bf16x8 (&fv)[2]) {
#pragma unroll
        for (int e = 0; e < 2; ++e) { d1 = MFMA32(fq[e], xf[e], d1); d2 = MFMA32(xf[e], fv[e], d2); }
        SBAR();
    };
#define LBAR() asm volatile("s_waitcnt lgkmcnt(0)\n\ts_barrier" ::: "memory")
    *(LDSP unsigned*)((lds_ptr)lds + AB_AUX + tid * 8) = (unsigned)w_r[tid] | ((unsigned)gate_w[tid] << 16);
    LBAR(); STAMP(0, 0);
    rd(0, 0, xa, qa, va);
#define A_STAGE(i) rd(i, 2, xb2, qb, vb2); mm(xa, qa, va); if ((i) < 7) { LBAR(); rd((i) + 1, 0, xa, qa, va); } mm(xb2, qb, vb2);
    A_STAGE(0) A_STAGE(1) A_STAGE(2) A_STAGE(3) A_STAGE(4) A_STAGE(5) A_STAGE(6) A_STAGE(7)
#undef A_STAGE
    STAMP(0, 1);
    u32x4 qw, kw, vw0, vw1;
#pragma unroll
    for (int i = 0; i < 4; ++i) {
        kw[i] = cvtpk(d1[8 + 2 * i], d1[8 + 2 * i + 1]);
        vw0[i] = cvtpk(d2[2 * i], d2[2 * i + 1]); vw1[i] = cvtpk(d2[8 + 2 * i], d2[8 + 2 * i + 1]);
    }
    const bool isv = r < 16;
    const unsigned vpos = h * 256 + swap23(r & 15) * 16;
    {
        char* kg = ws + WS_K + (size_t)head * 65536 + (8 * chunk + w) * 1024;
        char* vg = ws + WS_V + (size_t)head * 65536 + (8 * chunk + w) * 1024;
        st16_dev(lane * 16, kw, kg);
        if (isv) { st16_dev(vpos, vw0, vg); st16_dev(vpos + 512, vw1, vg); }
    }
#pragma unroll
    for (int i = 0; i < 4; ++i) qw[i] = cvtpk(d1[2 * i] * QSCALE, d1[2 * i + 1] * QSCALE);
    const bf16x8 qf = __builtin_bit_cast(bf16x8, qw);
    STAMP(0, 2);
    LBAR();
    STAMP(0, 3);
    *(LDSP u32x4*)((lds_ptr)lds + AB_K + w * 1024 + lane * 16) = kw;
    if (isv) { *(LDSP u32x4*)((lds_ptr)lds + AB_V + w * 1024 + vpos) = vw0; *(LDSP u32x4*)((lds_ptr)lds + AB_V + w * 1024 + vpos + 512) = vw1; }
    { u32x4 ones = {0x3F803F80u, 0x3F803F80u, 0x3F803F80u, 0x3F803F80u}; if (tid < 352) *(LDSP u32x4*)((lds_ptr)lds + AB_ONES + tid * 16) = ones; }
    const int tp = 4 * xcd + (loc & 3), ct = loc >> 2;
    const int tt = w & 1, cg = (w >> 1) & 1, kh = w >> 2;
    const int token = 64 * tp + 32 * tt + r, col = 64 * ct + 32 * cg + 16 * kh + 8 * h;
    const u32x4 xv = *(const u32x4*)(x + token * DM + col);
    LBAR();
    STAMP(0, 4);
    if (w == head) {
        LDSP float* rg = (LDSP float*)((lds_ptr)lds + AB_RG);
        if ((r & 30) == 16) {
#pragma unroll
            for (int qd = 0; qd < 4; ++qd) *(LDSP f32x4*)(rg + (r & 1) * 32 + 8 * qd + 4 * h) = f32x4{d2[4 * qd], d2[4 * qd + 1], d2[4 * qd + 2], d2[4 * qd + 3]};
        }
        const int token = 256 * chunk + 32 * w + r;
        const unsigned rb = cvtpk(rg[r], 0.f) & 0xFFFFu;
        unsigned key = (((rb & 0x8000u) ? (~rb & 0xFFFFu) : (rb | 0x8000u)) << 16) | (0xFFFFu - token);
#pragma unroll
        for (int o = 16; o >= 1; o >>= 1) { const unsigned t = __shfl_xor(key, o); key = t > key ? t : key; }
        if (lane == 0) st_dev(ws + WS_KEYS + (8 * chunk + w) * 8, (*(LDSP const u64*)((lds_ptr)lds + AB_TAG) << 32) | key);
        const float gs = rbf(rg[32 + r]);
        const float g = rbf(1.f / (1.f + __expf(-gs)));
        if (lane < 32) __hip_atomic_store((unsigned*)(ws + WS_GATE) + token, __float_as_uint(g), __ATOMIC_RELAXED, __HIP_MEMORY_SCOPE_AGENT);
    }
    lds_cptr kp = L + AB_K + lane * 16;
    lds_cptr vp = isv ? L + AB_V + h * 256 + r * 16 : L + AB_ONES + ((r >= 20 && r < 28) ? 64 : 0);
    const unsigned vinc = isv ? 4096u : 0u;
    f32x16 zero = {};
    bf16x8 kf = lds_rd128(kp);
    f32x16 s0 = MFMA32(kf, qf, zero);
    const float mref = max16(s0);
    f32x16 cneg;
#pragma unroll
    for (int i = 0; i < 16; ++i) { cneg[i] = -mref; s0[i] -= mref; }
    f32x16 o = {}, s1;
    kf = lds_rd128(kp + 1024);
    bf16x8 v0 = lds_rd128(vp), v1 = lds_rd128(vp + 512);
    u32x4 pa, pb = {0u, 0u, 0u, 0u};
#define EX(S, i) S[i] = __builtin_amdgcn_exp2f(S[i]); SBAR();
#define CV(P, S, i) P[i & 3] = cvtpk(S[2 * (i)], S[2 * (i) + 1]); SBAR();
#define STEP(SC, SN, u) \
        SN = MFMA32(kf, qf, cneg); SBAR(); \
        kf = lds_rd128(kq + ((u) + 2) * 1024); SBAR(); \
        EX(SC, 0) EX(SC, 1) EX(SC, 2) EX(SC, 3) \
        o = MFMA32(v1, __builtin_bit_cast(bf16x8, pb), o); SBAR(); \
        v1 = lds_rd128(vp + (u) * 1024 + 512); SBAR(); \
        CV(pa, SC, 0) EX(SC, 4) EX(SC, 5) CV(pa, SC, 1) EX(SC, 6) EX(SC, 7) CV(pa, SC, 2) EX(SC, 8) EX(SC, 9) CV(pa, SC, 3) EX(SC, 10) EX(SC, 11) \
        o = MFMA32(v0, __builtin_bit_cast(bf16x8, pa), o); SBAR(); \
        v0 = lds_rd128(vp + ((u) + 1) * 1024); SBAR(); \
        CV(pb, SC, 4) EX(SC, 12) EX(SC, 13) CV(pb, SC, 5) EX(SC, 14) EX(SC, 15) CV(pb, SC, 6) CV(pb, SC, 7)
    lds_cptr kq = kp;
    auto wait_words = [&](int off, unsigned p) -> unsigned {
        unsigned have = 0;
#pragma unroll 1
        for (unsigned spin = 0; spin < (1u << 20); ++spin) {
            const u32x4 v = *(LDSP volatile const u32x4*)((lds_ptr)lds + off);
            have = __builtin_amdgcn_readfirstlane(min(min(v[0], v[1]), min(v[2], v[3])));
            if (have >= p) break;
            __builtin_amdgcn_s_sleep(1);
        }
        asm volatile("" ::: "memory");
        return have;
    };
    unsigned kv_have = 0;
    auto landed = [&](unsigned p) { if (kv_have < p) kv_have = wait_words(AB_LAND, p); };
    const u64 tag = *(LDSP const u64*)((lds_ptr)lds + AB_TAG);
    asm volatile("s_waitcnt vmcnt(0)" ::: "memory");
    if (lane == 0) st_dev(ws + WS_FLAG_A + (head * 64 + 8 * chunk + w) * 64, tag);
    STAMP(0, 5);
#define PRIO_MODE 1
#define PRIO_K 5
    if (w < 4) __builtin_amdgcn_s_setprio(0); else __builtin_amdgcn_s_setprio(1);
    unsigned special = 0x2AAC | (w < 4 ? 1u << PRIO_K : 0u);
#pragma unroll 1
    for (int m = 0; m < 16; ++m) {
        if (__builtin_expect((special >> m) & 1, 0)) {
            if (m == PRIO_K && w < 4) __builtin_amdgcn_s_setprio(2);
            if (m == 2) {
                STAMP(1, 0);
                landed(1);
                STAMP(1, 1);
                s0 = MFMA32(lds_rd128(kq), qf, cneg); kf = lds_rd128(kq + 1024); v0 = lds_rd128(vp);
            } else if (m & 1) landed((m + 1) >> 1);
            if (kv_have >= 7) special &= (1u << PRIO_K);
        }
        SBAR();
        STEP(s0, s1, 0) STEP(s1, s0, 1)
        STEP(s0, s1, 2) STEP(s1, s0, 3)
        kq += 4096; vp += vinc;
    }
#undef STEP
#undef EX
#undef CV
    o = MFMA32(v1, __builtin_bit_cast(bf16x8, pb), o);
    STAMP(1, 2);
    float l = o[8];
    float chk = 0.f;
#pragma unroll
    for (int i = 0; i < 8; ++i) chk += fabsf(o[i]);
    const bool bad = !(l < 1.152921504606847e18f) || !(chk < 3.0e38f);
    if (__any(bad)) {
        float m = -INFINITY;
        o = f32x16{};
        lds_cptr vq = isv ? L + AB_V + h * 256 + r * 16 : L + AB_ONES;
        for (int t = 0; t < 64; ++t) {
            f32x16 s = MFMA32(lds_rd128(kp + t * 1024), qf, zero);
            const float mn = fmaxf(m, max16(s));
            const float f = __builtin_amdgcn_exp2f(m - mn);
            m = mn;
#pragma unroll
            for (int i = 0; i < 16; ++i) { s[i] -= mn; o[i] *= f; }
            bf16x8 p0, p1;
            exp_pack(s, p0, p1);
            lds_cptr vt = vq + (isv ? t * 1024 : 0);
            o = MFMA32(lds_rd128(vt), p0, o); o = MFMA32(lds_rd128(vt + 512), p1, o);
        }
        l = o[8];
    }
    const float inv = 1.f / l;
    u32x4 ov;
#pragma unroll
    for (int i = 0; i < 4; ++i) ov[i] = cvtpk(o[2 * i] * inv, o[2 * i + 1] * inv);
    st16_dev(lane * 16, ov, ws + WS_O + (size_t)((8 * chunk + w) * 32 + head) * 1024);
    asm volatile("s_waitcnt lgkmcnt(0)" ::: "memory");
    if (lane == 0) __hip_atomic_fetch_add((LDSP unsigned*)((lds_ptr)lds + AB_KVDONE), 1u, __ATOMIC_RELAXED, __HIP_MEMORY_SCOPE_WORKGROUP);
    wake_all();
    __builtin_amdgcn_s_setprio(0);
    asm volatile("s_waitcnt vmcnt(0)" ::: "memory");
    if (lane == 0) st_dev(ws + WS_FLAG_O + (head * 64 + 8 * chunk + w) * 64, tag);
    STAMP(1, 3);
    const unsigned wq3 = 8192 + cg * 4096 + (8 * h + q4) * 64 + blk * 32 + (((p4 & 1) << 1) | (p4 >> 1)) * 8 + kh * 2048;
    const unsigned oo3 = tt * 4096 + kh * 2048 + lane * 16;
    f32x16 acc = {};
    u32x4 y0, y1; unsigned gtb, idxb;
    lds_wait((LDSP unsigned*)((lds_ptr)lds + AB_SEEN), 1);
    asm volatile("global_load_dwordx4 %0, %4, %5 sc1\n\tglobal_load_dwordx4 %1, %4, %5 offset:16 sc1\n\tglobal_load_dword %2, %6, %7 sc1\n\tglobal_load_dword %3, %8, %5 offset:2048 sc1"
                 : "=&v"(y0), "=&v"(y1), "=&v"(gtb), "=&v"(idxb) : "v"(col * 4), "s"(ws + WS_Y2), "v"(token * 4), "s"(ws + WS_GATE), "v"(ct * 4) : "memory");
    {
        LDSP volatile const unsigned* lw = (LDSP volatile const unsigned*)((lds_ptr)lds + AB_LANDS) + (lane < 12 ? lane : 0);
        unsigned have = 0;
        auto need = [&](unsigned bits) {
            if ((have & bits) == bits) return;
#pragma unroll 1
            for (unsigned spin = 0; spin < (1u << 20); ++spin) {
                have = (unsigned)__ballot(*lw != 0u);
                if ((have & bits) == bits) break;
                __builtin_amdgcn_s_sleep(1);
            }
            asm volatile("" ::: "memory");
        };
        bf16x8 fa[4], fb[4];
        auto rd = [&](int s, bf16x8 (&f)[4]) { lds_cptr sb = L + s * 16384; f[0] = lds_rd128(sb + oo3); f[1] = lds_rd128(sb + oo3 + 1024); f[2] = lds_tr2(sb + wq3); f[3] = lds_tr2(sb + wq3 + 1024); SBAR(); };
        auto mm = [&](bf16x8 (&f)[4]) { acc = MFMA32(f[2], f[0], acc); acc = MFMA32(f[3], f[1], acc); SBAR(); };
        need(0xF01); rd(0, fa);
        need(0x002); rd(1, fb); mm(fa);
        need(0x004); rd(2, fa); mm(fb);
        need(0x008); rd(3, fb); mm(fa);
        need(0x010); rd(4, fa); mm(fb);
        need(0x020); rd(5, fb); mm(fa);
        need(0x040); rd(6, fa); mm(fb);
        need(0x080); rd(7, fb); mm(fa); mm(fb);
    }
    STAMP(1, 4);
    STAMP(4, 0);
    f32x4 k0, k1;
    {
        const f32x4 a0 = {acc[0], acc[1], acc[2], acc[3]}, a1 = {acc[4], acc[5], acc[6], acc[7]}, a2 = {acc[8], acc[9], acc[10], acc[11]}, a3 = {acc[12], acc[13], acc[14], acc[15]};
        LDSP f32x4* xw = (LDSP f32x4*)((lds_ptr)lds + AB_XCH + w * 2048) + lane;
        if (kh) { xw[0] = a0; xw[64] = a1; k0 = a2; k1 = a3; } else { xw[0] = a2; xw[64] = a3; k0 = a0; k1 = a1; }
    }
    STAMP(4, 1);
    asm volatile("s_waitcnt vmcnt(0) lgkmcnt(0)\n\ts_barrier" : "+v"(y0), "+v"(y1), "+v"(gtb), "+v"(idxb)::"memory");
    STAMP(4, 2);
    {
        LDSP const f32x4* xr = (LDSP const f32x4*)((lds_ptr)lds + AB_XCH + (w ^ 4) * 2048) + lane;
        const f32x4 a0 = k0 + xr[0], a1 = k1 + xr[64];
        const bool sel = token == (int)idxb;
        const float gt = __uint_as_float(gtb), omg = 1.f - gt;
        u32x4 res;
#pragma unroll
        for (int i = 0; i < 4; ++i) {
            float o2[2];
#pragma unroll
            for (int e = 0; e < 2; ++e) {
                const int c = 2 * i + e;
                const float a = c < 4 ? a0[c & 3] : a1[c & 3];
                const float xx = e ? __uint_as_float(xv[i] & 0xFFFF0000u) : __uint_as_float(xv[i] << 16);
                const float ys = __uint_as_float(c < 4 ? y0[c & 3] : y1[c & 3]);
                o2[e] = gt * a + omg * (xx + (sel ? ys : 0.f));
            }
            res[i] = cvtpk(o2[0], o2[1]);
        }
        STAMP(4, 3);
        st16_dev((token * DM + col) * 2, res, out);
    }
    STAMP(1, 5);
}

extern "C" void kernel_launch(void* const* d_in, const int* in_sizes, int n_in, void* d_out, int out_size, void* d_ws, size_t ws_size, hipStream_t stream) {
    const u16* x = (const u16*)d_in[0];
    const u16* wqkv = (const u16*)d_in[1];
    const u16* wo = (const u16*)d_in[2];
    const u16* w_r = (const u16*)d_in[4];
    const u16* wg = (const u16*)d_in[5];
    const u16* wu = (const u16*)d_in[6];
    const u16* wd = (const u16*)d_in[7];
    const u16* gate_w = (const u16*)d_in[8];
    char* ws = (char*)d_ws;
    kab<<<256, 768, 0, stream>>>(x, wqkv, w_r, gate_w, wg, wu, wd, wo, ws, (u16*)d_out);
}
```

```cpp
#include <hip/hip_runtime.h>
#include <hip/hip_bf16.h>
#include <stdint.h>

typedef __bf16 bf16x8 __attribute__((ext_vector_type(8)));
typedef __bf16 bf16x2 __attribute__((ext_vector_type(2)));
typedef short s16x4 __attribute__((ext_vector_type(4)));
typedef short s16x8 __attribute__((ext_vector_type(8)));
typedef float f32x16 __attribute__((ext_vector_type(16)));
typedef float f32x4 __attribute__((ext_vector_type(4)));
typedef float f32x2 __attribute__((ext_vector_type(2)));
typedef unsigned u32x4 __attribute__((ext_vector_type(4)));
typedef unsigned short u16;
#define LDSP __attribute__((address_space(3)))
typedef LDSP const char* lds_cptr;
typedef LDSP char* lds_ptr;
#define DI __device__ __forceinline__
#define MFMA32(a, b, c) __builtin_amdgcn_mfma_f32_32x32x16_bf16(a, b, c, 0, 0, 0)
#define SBAR() __builtin_amdgcn_sched_barrier(0)

#define MAPPING 0
#define STAMP(kid, i)
constexpr int SEQ = 2048, DM = 512, NH = 32, FF = 1376;
constexpr float QSCALE = 0.25f * 1.4426950408889634f;

constexpr size_t WS_Q = 0, WS_K = 2u << 20, WS_V = 4u << 20, WS_O = 6u << 20;
constexpr size_t WS_GATE = 8u << 20;
constexpr size_t WS_KEYS = WS_GATE + 8192;
constexpr size_t WS_H = WS_KEYS + 1024;
constexpr size_t WS_YP = WS_H + 8192;
constexpr size_t WS_EPOCH = 9u << 20;
constexpr size_t WS_FLAG_F = WS_EPOCH + 4096;
constexpr size_t WS_Y2 = WS_FLAG_F + 16384;
constexpr size_t WS_FLAG_Y = WS_Y2 + 4096;
constexpr size_t WS_FLAG_A = 10u << 20;
constexpr size_t WS_FLAG_O = WS_FLAG_A + 131072;

DI unsigned cvtpk(float lo, float hi) { f32x2 v = {lo, hi}; bf16x2 b = __builtin_convertvector(v, bf16x2); return __builtin_bit_cast(unsigned, b); }
DI float bf2f(unsigned b) { return __uint_as_float(b << 16); }
DI float rbf(float x) { return __uint_as_float(cvtpk(x, 0.f) << 16); }
DI unsigned lds_addr(const void* p) { return (unsigned)(size_t)(LDSP const char*)p; }
DI void glds16(unsigned voff, const void* sbase, unsigned ldsdst) {
    asm volatile("s_mov_b32 m0, %2\n\ts_nop 0\n\tglobal_load_lds_dwordx4 %0, %1" ::"v"(voff), "s"(sbase), "s"(ldsdst) : "memory");
}
DI bf16x8 lds_rd128(lds_cptr p) { return *(LDSP const bf16x8*)p; }
DI s16x4 lds_tr(lds_cptr p) { return __builtin_bit_cast(s16x4, __builtin_amdgcn_ds_read_tr16_b64_v4i16((LDSP s16x4*)p)); }
DI bf16x8 lds_tr2(lds_cptr p) {
    s16x4 a = lds_tr(p), b = lds_tr(p + 256);
    s16x8 r = {a[0], a[1], a[2], a[3], b[0], b[1], b[2], b[3]};
    return __builtin_bit_cast(bf16x8, r);
}
#define WAIT_BAR(N) asm volatile("s_waitcnt vmcnt(" #N ") lgkmcnt(0)\n\ts_barrier" ::: "memory")
DI int swap23(int v) { return (v & ~12) | ((v & 4) << 1) | ((v & 8) >> 1); }

constexpr int AB_SLOT = 39168, AB_W = 32768, AB_AUX = 156672;
constexpr int AB_K = 0, AB_V = 65536, AB_ONES = 131072;
constexpr int AB_FS = AB_ONES + 5632, AB_FH = AB_FS + 1024, AB_FC = AB_FH + 64;
constexpr int AB_XCH_ = AB_FS + 1536;
constexpr int AB_LAND = AB_FC + 16;
constexpr int AB_PROG = AB_LAND + 16;
constexpr int AB_LANDS = AB_FC + 64, AB_LANDW = AB_FC + 96;
constexpr int AB_KVDONE = AB_FC + 112, AB_SEEN = AB_FC + 116, AB_TAG = AB_FC + 128;
constexpr int AB_RG = AB_XCH_ + 16384;
constexpr int AB_XCH = AB_XCH_;
constexpr int AB_FW = AB_FS + 1280;
constexpr int AB_LDS = AB_AUX + 4096;
static_assert(AB_LDS <= 163840 && AB_FW + 16384 <= AB_LDS && AB_RG + 256 <= AB_AUX && AB_FW % 256 == 0, "LDS");

DI float max16(const f32x16& s) {
    float a = fmaxf(fmaxf(s[0], s[1]), s[2]), b = fmaxf(fmaxf(s[3], s[4]), s[5]);
    a = fmaxf(fmaxf(a, s[6]), s[7]); b = fmaxf(fmaxf(b, s[8]), s[9]);
    a = fmaxf(fmaxf(a, s[10]), s[11]); b = fmaxf(fmaxf(b, s[12]), s[13]);
    a = fmaxf(fmaxf(a, s[14]), s[15]);
    float m = fmaxf(a, b);
    auto rr = __builtin_amdgcn_permlane32_swap(__float_as_uint(m), __float_as_uint(m), false, false);
    return fmaxf(__uint_as_float(rr[0]), __uint_as_float(rr[1]));
}
DI void exp_pack(const f32x16& s, bf16x8& p0, bf16x8& p1) {
    u32x4 a, b;
#pragma unroll
    for (int i = 0; i < 4; ++i) a[i] = cvtpk(__builtin_amdgcn_exp2f(s[2 * i]), __builtin_amdgcn_exp2f(s[2 * i + 1]));
#pragma unroll
    for (int i = 0; i < 4; ++i) b[i] = cvtpk(__builtin_amdgcn_exp2f(s[8 + 2 * i]), __builtin_amdgcn_exp2f(s[8 + 2 * i + 1]));
    p0 = __builtin_bit_cast(bf16x8, a); p1 = __builtin_bit_cast(bf16x8, b);
}
DI void st16_dev(unsigned voff, u32x4 d, void* sbase) { asm volatile("global_store_dwordx4 %0, %1, %2 sc1\n\ts_nop 1" ::"v"(voff), "v"(d), "s"(sbase) : "memory"); }
DI void glds16_dev(unsigned voff, const void* sbase, unsigned ldsdst) {
    asm volatile("s_mov_b32 m0, %2\n\ts_nop 0\n\tglobal_load_lds_dwordx4 %0, %1 sc1" ::"v"(voff), "s"(sbase), "s"(ldsdst) : "memory");
}
typedef unsigned long long u64;
DI u64 ld_dev(const void* p) { return __hip_atomic_load((const u64*)p, __ATOMIC_RELAXED, __HIP_MEMORY_SCOPE_AGENT); }
DI void st_dev(void* p, u64 v) { __hip_atomic_store((u64*)p, v, __ATOMIC_RELAXED, __HIP_MEMORY_SCOPE_AGENT); }
DI void wait_flags(const char* flag, bool want, u64 tag) {
#pragma unroll 1
    for (unsigned spin = 0; spin < (1u << 20); ++spin) {
        const u64 v = want ? ld_dev(flag) : tag;
        if (__all(v == tag)) break;
        __builtin_amdgcn_s_sleep(1);
    }
}

#define BARRIER() asm volatile("s_barrier" ::: "memory")
#define WAIT_V(N) asm volatile("s_waitcnt vmcnt(" #N ")" ::: "memory")

DI void wait_flags_slow(const char* flag, bool want, u64 tag) {
#pragma unroll 1
    for (unsigned spin = 0; spin < (1u << 18); ++spin) {
        const u64 v = want ? ld_dev(flag) : tag;
        if (__all(v == tag)) break;
        __builtin_amdgcn_s_sleep(16);
    }
}
DI f32x4 mfma16(bf16x8 a, bf16x8 b, f32x4 c) { return __builtin_amdgcn_mfma_f32_16x16x32_bf16(a, b, c, 0, 0, 0); }
DI void lds_count(LDSP unsigned* c) { asm volatile("s_waitcnt vmcnt(0) lgkmcnt(0)" ::: "memory"); if ((threadIdx.x & 63) == 0) __hip_atomic_fetch_add(c, 1u, __ATOMIC_RELAXED, __HIP_MEMORY_SCOPE_WORKGROUP); }
DI void lds_wait(LDSP unsigned* c, unsigned n) {
#pragma unroll 1
    for (unsigned spin = 0; spin < (1u << 20); ++spin) {
        if (__hip_atomic_load(c, __ATOMIC_RELAXED, __HIP_MEMORY_SCOPE_WORKGROUP) >= n) break;
        __builtin_amdgcn_s_sleep(1);
    }
    asm volatile("" ::: "memory");
}
DI void lds_wait_idle(LDSP unsigned* c, unsigned n) {
    __builtin_amdgcn_s_setprio(0);
#pragma unroll 1
    for (unsigned spin = 0; spin < (1u << 20); ++spin) {
        if (__hip_atomic_load(c, __ATOMIC_RELAXED, __HIP_MEMORY_SCOPE_WORKGROUP) >= n) break;
        __builtin_amdgcn_s_sleep(8);
    }
    asm volatile("" ::: "memory");
    __builtin_amdgcn_s_setprio(3);
}
DI void lds_wait_lo(LDSP unsigned* c, unsigned n) {
    __builtin_amdgcn_s_setprio(0);
#pragma unroll 1
    for (unsigned spin = 0; spin < (1u << 18); ++spin) {
        if (__hip_atomic_load(c, __ATOMIC_RELAXED, __HIP_MEMORY_SCOPE_WORKGROUP) >= n) break;
        __builtin_amdgcn_s_sleep(6);
    }
    asm volatile("" ::: "memory");
    __builtin_amdgcn_s_setprio(3);
}
DI unsigned top_key_dev(char* ws, int lane, u64 tag) {
    u64 v = 0;
#pragma unroll 1
    for (unsigned spin = 0; spin < (1u << 20); ++spin) {
        v = ld_dev(ws + WS_KEYS + lane * 8);
        if (__all((unsigned)(v >> 32) == (unsigned)tag)) break;
        __builtin_amdgcn_s_sleep(8);
    }
    unsigned key = (unsigned)v;
#pragma unroll
    for (int o = 32; o >= 1; o >>= 1) { const unsigned t = __shfl_xor(key, o); key = t > key ? t : key; }
    return __builtin_amdgcn_readfirstlane(key);
}
DI bf16x8 tr_frag(lds_cptr p, int step) { s16x4 a = lds_tr(p), c = lds_tr(p + step); s16x8 t = {a[0], a[1], a[2], a[3], c[0], c[1], c[2], c[3]}; return __builtin_bit_cast(bf16x8, t); }
DI void ffn_unit(char* lds, const u16* x, const u16* wg, const u16* wu, const u16* wd, char* ws, int u, int j, int lane, u64 tag) {
    const int n = lane & 15, g = lane >> 4, q = n >> 2, p = n & 3;
    const unsigned slot = lds_addr(lds) + AB_FW + j * 4096;
    lds_cptr sl = (lds_cptr)lds + AB_FW + j * 4096;
    bf16x8 ag[4], au[4], ad[8];
    const unsigned vo = (lane >> 1) * (FF * 2) + (lane & 1) * 16;
    const size_t wo = (size_t)(128 * j) * (FF * 2) + 32 * u;
#pragma unroll
    for (int i = 0; i < 4; ++i) glds16(vo, (const char*)wg + wo + (size_t)i * 32 * (FF * 2), slot + i * 1024);
    asm volatile("s_waitcnt vmcnt(0)" ::: "memory");
#pragma unroll
    for (int ks = 0; ks < 4; ++ks) ag[ks] = tr_frag(sl + (32 * ks + 8 * g + q) * 32 + p * 8, 128);
    asm volatile("s_waitcnt lgkmcnt(0)" : "+v"(ag[0]), "+v"(ag[1]), "+v"(ag[2]), "+v"(ag[3])::"memory");
#pragma unroll
    for (int i = 0; i < 4; ++i) glds16(vo, (const char*)wu + wo + (size_t)i * 32 * (FF * 2), slot + i * 1024);
    asm volatile("s_waitcnt vmcnt(0)" ::: "memory");
#pragma unroll
    for (int ks = 0; ks < 4; ++ks) au[ks] = tr_frag(sl + (32 * ks + 8 * g + q) * 32 + p * 8, 128);
    asm volatile("s_waitcnt lgkmcnt(0)" : "+v"(au[0]), "+v"(au[1]), "+v"(au[2]), "+v"(au[3])::"memory");
#pragma unroll
    for (int i = 0; i < 4; ++i) glds16((lane >> 4) * 1024 + (lane & 15) * 16, (const char*)wd + (size_t)(16 * u + 4 * i) * 1024 + j * 256, slot + i * 1024);
    const unsigned key = top_key_dev(ws, lane, tag);
    const int idx = 0xFFFF - (key & 0xFFFF);
    bf16x8 xs[4];
#pragma unroll
    for (int ks = 0; ks < 4; ++ks) xs[ks] = *(const bf16x8*)(x + idx * DM + 128 * j + 32 * ks + 8 * g);
    asm volatile("s_waitcnt vmcnt(0)" ::: "memory");
#pragma unroll
    for (int t = 0; t < 8; ++t) ad[t] = tr_frag(sl + (8 * (g & 1) + q) * 256 + t * 32 + p * 8, 1024);
    f32x4 dg = {}, du = {};
#pragma unroll
    for (int ks = 0; ks < 4; ++ks) { dg = mfma16(ag[ks], xs[ks], dg); du = mfma16(au[ks], xs[ks], du); }
    LDSP float* fs = (LDSP float*)((lds_ptr)lds + AB_FS);
    LDSP unsigned* fc = (LDSP unsigned*)((lds_ptr)lds + AB_FC);
    if (n == 0) { *(LDSP f32x4*)(fs + j * 32 + 4 * g) = dg; *(LDSP f32x4*)(fs + j * 32 + 16 + 4 * g) = du; }
    lds_count(fc);
    if (j == 0) {
        lds_wait_lo(fc, 4);
        const int l32 = lane & 31;
        const float v = rbf((fs[l32] + fs[32 + l32]) + (fs[64 + l32] + fs[96 + l32]));
        const float up = __shfl(v, (lane & 15) + 16);
        const float hv = rbf(v / (1.f + __expf(-v))) * up;
        if (lane < 16) *(LDSP u16*)((lds_ptr)lds + AB_FH + lane * 2) = (u16)(cvtpk(hv, 0.f) & 0xFFFFu);
        lds_count(fc + 1);
    }
    lds_wait_lo(fc + 1, 1);
    u32x4 hb = *(LDSP const u32x4*)((lds_ptr)lds + AB_FH + (g & 1) * 16);
    if (g >= 2) hb = u32x4{0u, 0u, 0u, 0u};
    char* yb = ws + WS_YP + (size_t)u * 2048 + j * 512;
    f32x4 y[8];
#pragma unroll
    for (int t = 0; t < 8; ++t) y[t] = mfma16(ad[t], __builtin_bit_cast(bf16x8, hb), f32x4{});
    asm volatile("s_nop 7\n\ts_nop 1" : "+v"(y[0]), "+v"(y[1]), "+v"(y[2]), "+v"(y[3]), "+v"(y[4]), "+v"(y[5]), "+v"(y[6]), "+v"(y[7]));
    if (n == 0) {
#pragma unroll
        for (int t = 0; t < 8; ++t) st16_dev(t * 64 + g * 16, __builtin_bit_cast(u32x4, y[t]), yb);
    }
    lds_count(fc + 2);
    if (j == 0) { lds_wait_lo(fc + 2, 4); if (lane == 0) st_dev(ws + WS_FLAG_F + u * 128, tag); }
}
DI void ffn_reduce(char* lds, char* ws, int g8, int j, int lane, u64 tag) {
    const unsigned key = top_key_dev(ws, lane, tag);
    wait_flags_slow(ws + WS_FLAG_F + lane * 128, true, tag);
    wait_flags_slow(ws + WS_FLAG_F + (64 + lane) * 128, lane < 22, tag);
    const unsigned* yp = (const unsigned*)(ws + WS_YP) + 64 * g8 + lane;
    unsigned v[22];
#pragma unroll
    for (int i = 0; i < 22; ++i) { const int u = min(j + 4 * i, 85); v[i] = __hip_atomic_load(yp + u * DM, __ATOMIC_RELAXED, __HIP_MEMORY_SCOPE_AGENT); }
    float y = 0.f;
#pragma unroll
    for (int i = 0; i < 22; ++i) y += (j + 4 * i < 86) ? __uint_as_float(v[i]) : 0.f;
    LDSP float* fs = (LDSP float*)((lds_ptr)lds + AB_FS);
    LDSP unsigned* fc = (LDSP unsigned*)((lds_ptr)lds + AB_FC);
    fs[j * 64 + lane] = y;
    lds_count(fc);
    if (j == 0) {
        lds_wait_lo(fc, 4);
        const float ys = rbf((fs[lane] + fs[64 + lane]) + (fs[128 + lane] + fs[192 + lane]));
        const unsigned ob16 = key >> 16, rb = (ob16 & 0x8000u) ? (ob16 & 0x7FFFu) : (~ob16 & 0xFFFFu);
        const float rs = rbf(1.f / (1.f + __expf(-bf2f(rb))));
        __hip_atomic_store((unsigned*)(ws + WS_Y2) + 64 * g8 + lane, __float_as_uint(rbf(ys * rs)), __ATOMIC_RELAXED, __HIP_MEMORY_SCOPE_AGENT);
        if (lane == 0) __hip_atomic_store((unsigned*)(ws + WS_Y2) + 512 + g8, 0xFFFFu - (key & 0xFFFFu), __ATOMIC_RELAXED, __HIP_MEMORY_SCOPE_AGENT);
        asm volatile("s_waitcnt vmcnt(0)" ::: "memory");
        if (lane == 0) st_dev(ws + WS_FLAG_Y + g8 * 128, tag);
    }
}

DI void ab_loader(char* lds, const u16* x, const u16* wqkv, const u16* w_r, const u16* gate_w, const u16* wg, const u16* wu, const u16* wd, const u16* wo, char* ws, int head, int chunk, int j, int lane) {
    const unsigned lds0 = lds_addr(lds);
    const int rot = (head + chunk) & 7;
    __builtin_amdgcn_s_setprio(3);
    const unsigned xv0 = (lane >> 3) * 1024 + ((lane & 7) ^ ((lane >> 4) & 7)) * 16;
    const unsigned xv1 = (lane >> 3) * 1024 + ((lane & 7) ^ (((lane >> 4) + 4) & 7)) * 16;
    unsigned wsrc[2];
#pragma unroll
    for (int e = 0; e < 2; ++e) {
        const int o = 768 * (2 * j + e) + 16 * lane;
        if (o < 4096) { const int cc = (o >> 4) & 3; wsrc[e] = (o >> 6) * 3072 + (cc < 2 ? 16 * cc : 1024 + 16 * (cc - 2)); }
        else { const int o2 = o - 4096, row = min(o2 >> 5, 63); wsrc[e] = row * 3072 + 2048 + 16 * ((o2 >> 4) & 1); }
    }
    const char* xb = (const char*)x + (size_t)(256 * chunk + 64 * j) * 1024;
    const char* wb = (const char*)wqkv + head * 32;
    auto issue = [&](int i) {
        const int s = (rot + i) & 7;
        const unsigned sl = lds0 + ((i + 1) % 3) * AB_SLOT;
        if (lane < 48) { glds16(wsrc[0], wb + (size_t)s * 64 * 3072, sl + AB_W + 768 * (2 * j)); glds16(wsrc[1], wb + (size_t)s * 64 * 3072, sl + AB_W + 768 * (2 * j + 1)); }
#pragma unroll
        for (int q = 0; q < 8; ++q) glds16((q & 1) ? xv1 : xv0, xb + q * 8192 + s * 128, sl + (8 * j + q) * 1024);
    };
    STAMP(3, 1); issue(0); issue(1);
    unsigned e_lo, e_hi;
    asm volatile("global_load_dword %0, %2, %3 sc1\n\tglobal_load_dword %1, %2, %3 offset:4 sc1" : "=&v"(e_lo), "=&v"(e_hi) : "v"(0u), "s"(ws + WS_EPOCH) : "memory");
    WAIT_BAR(12); STAMP(3, 2); issue(2);
    asm volatile("s_waitcnt vmcnt(10)\n\ts_barrier" : "+v"(e_lo), "+v"(e_hi)::"memory");
    const u64 tag = (((u64)__builtin_amdgcn_readfirstlane(e_hi) << 32) | __builtin_amdgcn_readfirstlane(e_lo)) + 1;
    if (j == 0 && lane == 0) *(LDSP u64*)((lds_ptr)lds + AB_TAG) = tag;
    issue(3);
    WAIT_BAR(10); issue(4);
    WAIT_BAR(10); issue(5);
    WAIT_BAR(10); issue(6);
    WAIT_BAR(10); issue(7);
    WAIT_BAR(10);
    WAIT_BAR(0);
    BARRIER();
    wait_flags(ws + WS_FLAG_A + (head * 64 + ((chunk + 1 + (lane >> 1)) & 7) * 8 + 2 * j + (lane & 1)) * 64, lane < 14, tag);
    STAMP(2, 0);
    {
        const char* kb = ws + WS_K + (size_t)head * 65536 + j * 2048;
        const char* vb = ws + WS_V + (size_t)head * 65536 + j * 2048;
        auto fetch = [&](int p) {
            const int cp = (chunk + p) & 7;
            glds16_dev(lane * 16, kb + cp * 8192, lds0 + AB_K + p * 8192 + j * 2048); glds16_dev(lane * 16, kb + cp * 8192 + 1024, lds0 + AB_K + p * 8192 + j * 2048 + 1024);
            glds16_dev(lane * 16, vb + cp * 8192, lds0 + AB_V + p * 8192 + j * 2048); glds16_dev(lane * 16, vb + cp * 8192 + 1024, lds0 + AB_V + p * 8192 + j * 2048 + 1024);
        };
        LDSP volatile unsigned* land = (LDSP volatile unsigned*)((lds_ptr)lds + AB_LAND) + j;
#define LANDED(N, p) do { WAIT_V(N); if (lane == 0) *land = p; } while (0)
        fetch(1); fetch(2);
        LANDED(4, 1);
        STAMP(2, 1);
        fetch(3); fetch(4); fetch(5); fetch(6); fetch(7);
        LANDED(20, 2); LANDED(16, 3); LANDED(12, 4); LANDED(8, 5); LANDED(4, 6); LANDED(0, 7);
#undef LANDED
        STAMP(2, 2);
    }
    __builtin_amdgcn_s_setprio(0);
    const int ce = blockIdx.x & 7, tp = 4 * ce + ((blockIdx.x >> 3) & 3), ct = blockIdx.x >> 5;
    const char* wb3 = (const char*)wo + (size_t)((j & 1) * 32) * 1024 + ct * 128 + (j >> 1) * 64;
    const unsigned wvo3 = (lane >> 2) * 1024 + (lane & 3) * 16;
    {
        u32x4 d0, d1, d2, d3;
        const unsigned pv = (128 * ((blockIdx.x >> 3) & 3) + 32 * j + (lane >> 3)) * 1024 + ct * 128 + (lane & 7) * 16;
        asm volatile("global_load_dwordx4 %0, %4, %8\n\tglobal_load_dwordx4 %1, %5, %8\n\tglobal_load_dwordx4 %2, %6, %8\n\tglobal_load_dwordx4 %3, %7, %8\n\ts_waitcnt vmcnt(0)"
                     : "=&v"(d0), "=&v"(d1), "=&v"(d2), "=&v"(d3) : "v"(pv), "v"(pv + 8192), "v"(pv + 16384), "v"(pv + 24576), "s"(wo) : "memory");
    }
    if (blockIdx.x < 94) {
        __builtin_amdgcn_s_setprio(3);
        if (blockIdx.x < 86) ffn_unit(lds, x, wg, wu, wd, ws, blockIdx.x, j, lane, tag);
        else ffn_reduce(lds, ws, blockIdx.x - 86, j, lane, tag);
        __builtin_amdgcn_s_setprio(0);
    }
    STAMP(2, 3);
    if (blockIdx.x == 255 && j == 1) {
#pragma unroll
        for (int i = 0; i < 4; ++i) wait_flags_slow(ws + WS_FLAG_A + (64 * i + lane) * 512, true, tag);
        if (lane == 0) st_dev(ws + WS_EPOCH, tag);
    }
    if (j == 0) {
        const int T = 2 * tp + (lane & 1);
        wait_flags_slow(lane < 2 ? ws + WS_FLAG_A + ((T & 7) * 64 + T) * 64 : ws + WS_FLAG_Y + ct * 128, lane <= 2, tag);
        if (lane == 0) *(LDSP volatile unsigned*)((lds_ptr)lds + AB_SEEN) = 1u;
    }
    lds_wait_idle((LDSP unsigned*)((lds_ptr)lds + AB_KVDONE), 8);
    STAMP(2, 4);
#pragma unroll
    for (int s = 0; s < 8; ++s)
#pragma unroll
        for (int q = 0; q < 2; ++q) glds16(wvo3, wb3 + (size_t)(64 * s + 16 * q) * 1024, lds0 + s * 16384 + 8192 + (2 * j + q) * 1024);
    {
        const char* fl = ws + WS_FLAG_O + ((4 * (j + 4 * ((lane >> 3) & 1)) + ((lane >> 1) & 3)) * 64 + 2 * tp + (lane & 1)) * 64;
        unsigned todo = 3;
        bool first = true;
#pragma unroll 1
        for (unsigned spin = 0; spin < (1u << 20) && todo; ++spin) {
            const u64 v = ld_dev(fl);
            const unsigned long long ok = __ballot(v == tag);
            if (first) { first = false; if (lane == 0) *((LDSP volatile unsigned*)((lds_ptr)lds + AB_LANDW) + j) = 1u; }
            const unsigned rdy = (((ok & 0xFFull) == 0xFFull ? 1u : 0u) | ((ok & 0xFF00ull) == 0xFF00ull ? 2u : 0u)) & todo;
            if (rdy) {
#pragma unroll
                for (int e = 0; e < 2; ++e) if (rdy & (1u << e)) {
                    const int s = j + 4 * e;
                    const char* ob = ws + WS_O + (size_t)(2 * tp) * 32768 + s * 4096;
#pragma unroll
                    for (int q = 0; q < 8; ++q) glds16_dev(lane * 16, ob + (q >> 2) * 32768 + (q & 3) * 1024, lds0 + s * 16384 + q * 1024);
                }
                WAIT_V(0);
                if (lane < 2 && (rdy & (1u << lane))) *((LDSP volatile unsigned*)((lds_ptr)lds + AB_LANDS) + j + 4 * lane) = 1u;
                todo &= ~rdy;
            } else __builtin_amdgcn_s_sleep(1);
        }
    }
    STAMP(2, 5);
}

__global__ __launch_bounds__(768) void kab(const u16* __restrict__ x, const u16* __restrict__ wqkv, const u16* __restrict__ w_r, const u16* __restrict__ gate_w,
                                          const u16* __restrict__ wg, const u16* __restrict__ wu, const u16* __restrict__ wd, const u16* __restrict__ wo,
                                          char* __restrict__ ws, u16* __restrict__ out) {
    __shared__ __attribute__((aligned(16))) char lds[AB_LDS];
    const int tid = threadIdx.x, lane = tid & 63, w = __builtin_amdgcn_readfirstlane(tid >> 6);
    const int b = blockIdx.x, xcd = b & 7, loc = b >> 3;
    const int head = xcd * 4 + (loc >> 3), chunk = loc & 7;
    if (w >= 8) { ab_loader(lds, x, wqkv, w_r, gate_w, wg, wu, wd, wo, ws, head, chunk, w - 8, lane); return; }
    const int r = lane & 31, h = lane >> 5;
    lds_cptr L = (lds_cptr)lds;
    const int rot = (head + chunk) & 7;
    unsigned xo[4];
#pragma unroll
    for (int ks = 0; ks < 4; ++ks) xo[ks] = (32 * w + r) * 128 + ((2 * ks + h) ^ ((r >> 1) & 7)) * 16;
    const int blk = (lane >> 4) & 1, q4 = (lane & 15) >> 2, p4 = lane & 3;
    const unsigned wq = AB_W + (8 * h + q4) * 64 + blk * 32 + (((p4 & 1) << 1) | (p4 >> 1)) * 8;
    const unsigned wva = blk ? (8 * h + q4) * 8 : (8 * h + q4) * 32 + p4 * 8, wvk = blk ? 128 : 512;
    f32x16 d1 = {}, d2 = {};
    bf16x8 xa[2], qa[2], va[2], xb2[2], qb[2], vb2[2];
    auto rd = [&](int i, int k0, bf16x8 (&xf)[2], bf16x8 (&fq)[2], bf16x8 (&fv)[2]) {
        const int s = (rot + i) & 7;
        lds_cptr sl = L + ((i + 1) % 3) * AB_SLOT;
        lds_cptr vv = L + wva + (blk ? AB_AUX + s * 512 : ((i + 1) % 3) * AB_SLOT + AB_W + 4096);
#pragma unroll
        for (int e = 0; e < 2; ++e) {
            const int ks = k0 + e;
            xf[e] = lds_rd128(sl + xo[ks]);
            fq[e] = lds_tr2(sl + wq + ks * 1024);
            s16x4 a = lds_tr(vv + ks * wvk), c = lds_tr(vv + ks * wvk + (wvk >> 2));
            s16x8 t = {a[0], a[1], a[2], a[3], c[0], c[1], c[2], c[3]};
            fv[e] = __builtin_bit_cast(bf16x8, t);
        }
        SBAR();
    };
    auto mm = [&](bf16x8 (&xf)[2], bf16x8 (&fq)[2], bf16x8 (&fv)[2]) {
#pragma unroll
        for (int e = 0; e < 2; ++e) { d1 = MFMA32(fq[e], xf[e], d1); d2 = MFMA32(xf[e], fv[e], d2); }
        SBAR();
    };
#define LBAR() asm volatile("s_waitcnt lgkmcnt(0)\n\ts_barrier" ::: "memory")
    { u32x4 ones = {0x3F803F80u, 0x3F803F80u, 0x3F803F80u, 0x3F803F80u}; if (tid < 352) *(LDSP u32x4*)((lds_ptr)lds + AB_ONES + tid * 16) = ones; }
    if (tid < 32) *(LDSP unsigned*)((lds_ptr)lds + AB_FC + tid * 4) = 0u;
    *(LDSP unsigned*)((lds_ptr)lds + AB_AUX + tid * 8) = (unsigned)w_r[tid] | ((unsigned)gate_w[tid] << 16);
    LBAR(); STAMP(0, 0);
    rd(0, 0, xa, qa, va);
#define A_STAGE(i) rd(i, 2, xb2, qb, vb2); mm(xa, qa, va); if ((i) < 7) { LBAR(); rd((i) + 1, 0, xa, qa, va); } mm(xb2, qb, vb2);
    A_STAGE(0) A_STAGE(1) A_STAGE(2) A_STAGE(3) A_STAGE(4) A_STAGE(5) A_STAGE(6) A_STAGE(7)
#undef A_STAGE
    STAMP(0, 1);
    u32x4 qw, kw, vw0, vw1;
#pragma unroll
    for (int i = 0; i < 4; ++i) {
        kw[i] = cvtpk(d1[8 + 2 * i], d1[8 + 2 * i + 1]);
        vw0[i] = cvtpk(d2[2 * i], d2[2 * i + 1]); vw1[i] = cvtpk(d2[8 + 2 * i], d2[8 + 2 * i + 1]);
    }
    const bool isv = r < 16;
    const unsigned vpos = h * 256 + swap23(r & 15) * 16;
    {
        char* kg = ws + WS_K + (size_t)head * 65536 + (8 * chunk + w) * 1024;
        char* vg = ws + WS_V + (size_t)head * 65536 + (8 * chunk + w) * 1024;
        st16_dev(lane * 16, kw, kg);
        if (isv) { st16_dev(vpos, vw0, vg); st16_dev(vpos + 512, vw1, vg); }
    }
    *(LDSP u32x4*)((lds_ptr)lds + AB_K + w * 1024 + lane * 16) = kw;
    if (isv) { *(LDSP u32x4*)((lds_ptr)lds + AB_V + w * 1024 + vpos) = vw0; *(LDSP u32x4*)((lds_ptr)lds + AB_V + w * 1024 + vpos + 512) = vw1; }
#pragma unroll
    for (int i = 0; i < 4; ++i) qw[i] = cvtpk(d1[2 * i] * QSCALE, d1[2 * i + 1] * QSCALE);
    const bf16x8 qf = __builtin_bit_cast(bf16x8, qw);
    f32x16 zero = {};
    const float mref = max16(MFMA32(__builtin_bit_cast(bf16x8, kw), qf, zero));
    f32x16 cneg;
#pragma unroll
    for (int i = 0; i < 16; ++i) cneg[i] = -mref;
    STAMP(0, 2);
    const int tp = 4 * xcd + (loc & 3), ct = loc >> 2;
    const int tt = w & 1, cg = (w >> 1) & 1, kh = w >> 2;
    const int token = 64 * tp + 32 * tt + r, col = 64 * ct + 32 * cg + 16 * kh + 8 * h;
    const u32x4 xv = *(const u32x4*)(x + token * DM + col);
    LBAR();
    STAMP(0, 4);
    if (w == head) {
        LDSP float* rg = (LDSP float*)((lds_ptr)lds + AB_RG);
        if ((r & 30) == 16) {
#pragma unroll
            for (int qd = 0; qd < 4; ++qd) *(LDSP f32x4*)(rg + (r & 1) * 32 + 8 * qd + 4 * h) = f32x4{d2[4 * qd], d2[4 * qd + 1], d2[4 * qd + 2], d2[4 * qd + 3]};
        }
        const int token = 256 * chunk + 32 * w + r;
        const unsigned rb = cvtpk(rg[r], 0.f) & 0xFFFFu;
        unsigned key = (((rb & 0x8000u) ? (~rb & 0xFFFFu) : (rb | 0x8000u)) << 16) | (0xFFFFu - token);
#pragma unroll
        for (int o = 16; o >= 1; o >>= 1) { const unsigned t = __shfl_xor(key, o); key = t > key ? t : key; }
        if (lane == 0) st_dev(ws + WS_KEYS + (8 * chunk + w) * 8, (*(LDSP const u64*)((lds_ptr)lds + AB_TAG) << 32) | key);
        const float gs = rbf(rg[32 + r]);
        const float g = rbf(1.f / (1.f + __expf(-gs)));
        if (lane < 32) __hip_atomic_store((unsigned*)(ws + WS_GATE) + token, __float_as_uint(g), __ATOMIC_RELAXED, __HIP_MEMORY_SCOPE_AGENT);
    }
    lds_cptr kp = L + AB_K + lane * 16;
    lds_cptr vp = isv ? L + AB_V + h * 256 + r * 16 : L + AB_ONES + ((r >= 20 && r < 28) ? 64 : 0);
    const unsigned vinc = isv ? 4096u : 0u;
    bf16x8 kf = lds_rd128(kp);
    f32x16 s0 = MFMA32(kf, qf, cneg);
    f32x16 o = {}, s1;
    kf = lds_rd128(kp + 1024);
    bf16x8 v0 = lds_rd128(vp), v1 = lds_rd128(vp + 512);
    u32x4 pa, pb = {0u, 0u, 0u, 0u};
#define EX(S, i) S[i] = __builtin_amdgcn_exp2f(S[i]); SBAR();
#define CV(P, S, i) P[i & 3] = cvtpk(S[2 * (i)], S[2 * (i) + 1]); SBAR();
#define STEP(SC, SN, u) \
        SN = MFMA32(kf, qf, cneg); SBAR(); \
        kf = lds_rd128(kq + ((u) + 2) * 1024); SBAR(); \
        EX(SC, 0) EX(SC, 1) EX(SC, 2) EX(SC, 3) \
        o = MFMA32(v1, __builtin_bit_cast(bf16x8, pb), o); SBAR(); \
        v1 = lds_rd128(vp + (u) * 1024 + 512); SBAR(); \
        CV(pa, SC, 0) EX(SC, 4) EX(SC, 5) CV(pa, SC, 1) EX(SC, 6) EX(SC, 7) CV(pa, SC, 2) EX(SC, 8) EX(SC, 9) CV(pa, SC, 3) EX(SC, 10) EX(SC, 11) \
        o = MFMA32(v0, __builtin_bit_cast(bf16x8, pa), o); SBAR(); \
        v0 = lds_rd128(vp + ((u) + 1) * 1024); SBAR(); \
        CV(pb, SC, 4) EX(SC, 12) EX(SC, 13) CV(pb, SC, 5) EX(SC, 14) EX(SC, 15) CV(pb, SC, 6) CV(pb, SC, 7)
    lds_cptr kq = kp;
    auto wait_words = [&](int off, unsigned p) -> unsigned {
        unsigned have = 0;
#pragma unroll 1
        for (unsigned spin = 0; spin < (1u << 20); ++spin) {
            const u32x4 v = *(LDSP volatile const u32x4*)((lds_ptr)lds + off);
            have = __builtin_amdgcn_readfirstlane(min(min(v[0], v[1]), min(v[2], v[3])));
            if (have >= p) break;
            __builtin_amdgcn_s_sleep(1);
        }
        asm volatile("" ::: "memory");
        return have;
    };
    unsigned kv_have = 0;
    auto landed = [&](unsigned p) { if (kv_have < p) kv_have = wait_words(AB_LAND, p); };
    const u64 tag = *(LDSP const u64*)((lds_ptr)lds + AB_TAG);
    asm volatile("s_waitcnt vmcnt(0)" ::: "memory");
    if (lane == 0) st_dev(ws + WS_FLAG_A + (head * 64 + 8 * chunk + w) * 64, tag);
    STAMP(0, 5);
#define PRIO_MODE 1
#define PRIO_K 5
    if (w < 4) __builtin_amdgcn_s_setprio(0); else __builtin_amdgcn_s_setprio(1);
    unsigned special = 0x2AAC | (w < 4 ? 1u << PRIO_K : 0u);
#pragma unroll 1
    for (int m = 0; m < 16; ++m) {
        if (__builtin_expect((special >> m) & 1, 0)) {
            if (m == PRIO_K && w < 4) __builtin_amdgcn_s_setprio(2);
            if (m == 2) {
                STAMP(1, 0);
                landed(1);
                STAMP(1, 1);
                s0 = MFMA32(lds_rd128(kq), qf, cneg); kf = lds_rd128(kq + 1024); v0 = lds_rd128(vp);
            } else if (m & 1) landed((m + 1) >> 1);
            if (kv_have >= 7) special &= (1u << PRIO_K);
        }
        SBAR();
        STEP(s0, s1, 0) STEP(s1, s0, 1)
        STEP(s0, s1, 2) STEP(s1, s0, 3)
        kq += 4096; vp += vinc;
    }
#undef STEP
#undef EX
#undef CV
    o = MFMA32(v1, __builtin_bit_cast(bf16x8, pb), o);
    STAMP(1, 2);
    float l = o[8];
    float chk = 0.f;
#pragma unroll
    for (int i = 0; i < 8; ++i) chk += fabsf(o[i]);
    const bool bad = !(l < 1.152921504606847e18f) || !(chk < 3.0e38f);
    if (__any(bad)) {
        float m = -INFINITY;
        o = f32x16{};
        lds_cptr vq = isv ? L + AB_V + h * 256 + r * 16 : L + AB_ONES;
        for (int t = 0; t < 64; ++t) {
            f32x16 s = MFMA32(lds_rd128(kp + t * 1024), qf, zero);
            const float mn = fmaxf(m, max16(s));
            const float f = __builtin_amdgcn_exp2f(m - mn);
            m = mn;
#pragma unroll
            for (int i = 0; i < 16; ++i) { s[i] -= mn; o[i] *= f; }
            bf16x8 p0, p1;
            exp_pack(s, p0, p1);
            lds_cptr vt = vq + (isv ? t * 1024 : 0);
            o = MFMA32(lds_rd128(vt), p0, o); o = MFMA32(lds_rd128(vt + 512), p1, o);
        }
        l = o[8];
    }
    const float inv = 1.f / l;
    u32x4 ov;
#pragma unroll
    for (int i = 0; i < 4; ++i) ov[i] = cvtpk(o[2 * i] * inv, o[2 * i + 1] * inv);
    st16_dev(lane * 16, ov, ws + WS_O + (size_t)((8 * chunk + w) * 32 + head) * 1024);
    asm volatile("s_waitcnt lgkmcnt(0)" ::: "memory");
    if (lane == 0) __hip_atomic_fetch_add((LDSP unsigned*)((lds_ptr)lds + AB_KVDONE), 1u, __ATOMIC_RELAXED, __HIP_MEMORY_SCOPE_WORKGROUP);
    __builtin_amdgcn_s_setprio(0);
    asm volatile("s_waitcnt vmcnt(0)" ::: "memory");
    if (lane == 0) st_dev(ws + WS_FLAG_O + (head * 64 + 8 * chunk + w) * 64, tag);
    STAMP(1, 3);
    const unsigned wq3 = 8192 + cg * 4096 + (8 * h + q4) * 64 + blk * 32 + (((p4 & 1) << 1) | (p4 >> 1)) * 8 + kh * 2048;
    const unsigned oo3 = tt * 4096 + kh * 2048 + lane * 16;
    f32x16 acc = {};
    u32x4 y0, y1; unsigned gtb, idxb;
    lds_wait((LDSP unsigned*)((lds_ptr)lds + AB_SEEN), 1);
    asm volatile("global_load_dwordx4 %0, %4, %5 sc1\n\tglobal_load_dwordx4 %1, %4, %5 offset:16 sc1\n\tglobal_load_dword %2, %6, %7 sc1\n\tglobal_load_dword %3, %8, %5 offset:2048 sc1"
                 : "=&v"(y0), "=&v"(y1), "=&v"(gtb), "=&v"(idxb) : "v"(col * 4), "s"(ws + WS_Y2), "v"(token * 4), "s"(ws + WS_GATE), "v"(ct * 4) : "memory");
    {
        LDSP volatile const unsigned* lw = (LDSP volatile const unsigned*)((lds_ptr)lds + AB_LANDS) + (lane < 12 ? lane : 0);
        unsigned have = 0;
        auto need = [&](unsigned bits) {
            if ((have & bits) == bits) return;
#pragma unroll 1
            for (unsigned spin = 0; spin < (1u << 20); ++spin) {
                have = (unsigned)__ballot(*lw != 0u);
                if ((have & bits) == bits) break;
                __builtin_amdgcn_s_sleep(1);
            }
            asm volatile("" ::: "memory");
        };
        bf16x8 fa[4], fb[4];
        auto rd = [&](int s, bf16x8 (&f)[4]) { lds_cptr sb = L + s * 16384; f[0] = lds_rd128(sb + oo3); f[1] = lds_rd128(sb + oo3 + 1024); f[2] = lds_tr2(sb + wq3); f[3] = lds_tr2(sb + wq3 + 1024); SBAR(); };
        auto mm = [&](bf16x8 (&f)[4]) { acc = MFMA32(f[2], f[0], acc); acc = MFMA32(f[3], f[1], acc); SBAR(); };
        need(0xF01); rd(0, fa);
        need(0x002); rd(1, fb); mm(fa);
        need(0x004); rd(2, fa); mm(fb);
        need(0x008); rd(3, fb); mm(fa);
        need(0x010); rd(4, fa); mm(fb);
        need(0x020); rd(5, fb); mm(fa);
        need(0x040); rd(6, fa); mm(fb);
        need(0x080); rd(7, fb); mm(fa); mm(fb);
    }
    STAMP(1, 4);
    STAMP(4, 0);
    f32x4 k0, k1;
    {
        const f32x4 a0 = {acc[0], acc[1], acc[2], acc[3]}, a1 = {acc[4], acc[5], acc[6], acc[7]}, a2 = {acc[8], acc[9], acc[10], acc[11]}, a3 = {acc[12], acc[13], acc[14], acc[15]};
        LDSP f32x4* xw = (LDSP f32x4*)((lds_ptr)lds + AB_XCH + w * 2048) + lane;
        if (kh) { xw[0] = a0; xw[64] = a1; k0 = a2; k1 = a3; } else { xw[0] = a2; xw[64] = a3; k0 = a0; k1 = a1; }
    }
    STAMP(4, 1);
    asm volatile("s_waitcnt vmcnt(0) lgkmcnt(0)\n\ts_barrier" : "+v"(y0), "+v"(y1), "+v"(gtb), "+v"(idxb)::"memory");
    STAMP(4, 2);
    {
        LDSP const f32x4* xr = (LDSP const f32x4*)((lds_ptr)lds + AB_XCH + (w ^ 4) * 2048) + lane;
        const f32x4 a0 = k0 + xr[0], a1 = k1 + xr[64];
        const bool sel = token == (int)idxb;
        const float gt = __uint_as_float(gtb), omg = 1.f - gt;
        u32x4 res;
#pragma unroll
        for (int i = 0; i < 4; ++i) {
            float o2[2];
#pragma unroll
            for (int e = 0; e < 2; ++e) {
                const int c = 2 * i + e;
                const float a = c < 4 ? a0[c & 3] : a1[c & 3];
                const float xx = e ? __uint_as_float(xv[i] & 0xFFFF0000u) : __uint_as_float(xv[i] << 16);
                const float ys = __uint_as_float(c < 4 ? y0[c & 3] : y1[c & 3]);
                o2[e] = gt * a + omg * (xx + (sel ? ys : 0.f));
            }
            res[i] = cvtpk(o2[0], o2[1]);
        }
        STAMP(4, 3);
        st16_dev((token * DM + col) * 2, res, out);
    }
    STAMP(1, 5);
}

extern "C" void kernel_launch(void* const* d_in, const int* in_sizes, int n_in, void* d_out, int out_size, void* d_ws, size_t ws_size, hipStream_t stream) {
    const u16* x = (const u16*)d_in[0];
    const u16* wqkv = (const u16*)d_in[1];
    const u16* wo = (const u16*)d_in[2];
    const u16* w_r = (const u16*)d_in[4];
    const u16* wg = (const u16*)d_in[5];
    const u16* wu = (const u16*)d_in[6];
    const u16* wd = (const u16*)d_in[7];
    const u16* gate_w = (const u16*)d_in[8];
    char* ws = (char*)d_ws;
    kab<<<256, 768, 0, stream>>>(x, wqkv, w_r, gate_w, wg, wu, wd, wo, ws, (u16*)d_out);
}
```

```cpp
#include <hip/hip_runtime.h>
#include <hip/hip_bf16.h>
#include <stdint.h>

typedef __bf16 bf16x8 __attribute__((ext_vector_type(8)));
typedef __bf16 bf16x2 __attribute__((ext_vector_type(2)));
typedef short s16x4 __attribute__((ext_vector_type(4)));
typedef short s16x8 __attribute__((ext_vector_type(8)));
typedef float f32x16 __attribute__((ext_vector_type(16)));
typedef float f32x4 __attribute__((ext_vector_type(4)));
typedef float f32x2 __attribute__((ext_vector_type(2)));
typedef unsigned u32x4 __attribute__((ext_vector_type(4)));
typedef unsigned short u16;
#define LDSP __attribute__((address_space(3)))
typedef LDSP const char* lds_cptr;
typedef LDSP char* lds_ptr;
#define DI __device__ __forceinline__
#define MFMA32(a, b, c) __builtin_amdgcn_mfma_f32_32x32x16_bf16(a, b, c, 0, 0, 0)
#define SBAR() __builtin_amdgcn_sched_barrier(0)

constexpr int DM = 512, FF = 1376;
constexpr float QSCALE = 0.25f * 1.4426950408889634f;

constexpr size_t WS_K = 2u << 20;
constexpr size_t WS_V = 4u << 20;
constexpr size_t WS_O = 6u << 20;
constexpr size_t WS_GATE = 8u << 20;
constexpr size_t WS_KEYS = WS_GATE + 16384;
constexpr size_t WS_YP = WS_KEYS + 9216;
constexpr size_t WS_EPOCH = 9u << 20;
constexpr size_t WS_FLAG_F = WS_EPOCH + 4096;
constexpr size_t WS_Y2 = WS_FLAG_F + 16384;
constexpr size_t WS_FLAG_Y = WS_Y2 + 4096;
constexpr size_t WS_FLAG_A = 10u << 20;
constexpr size_t WS_FLAG_O = WS_FLAG_A + 131072;

DI unsigned cvtpk(float lo, float hi) { f32x2 v = {lo, hi}; bf16x2 b = __builtin_convertvector(v, bf16x2); return __builtin_bit_cast(unsigned, b); }
DI float bf2f(unsigned b) { return __uint_as_float(b << 16); }
DI float rbf(float x) { return __uint_as_float(cvtpk(x, 0.f) << 16); }
DI unsigned lds_addr(const void* p) { return (unsigned)(size_t)(LDSP const char*)p; }
DI void glds16(unsigned voff, const void* sbase, unsigned ldsdst) {
    asm volatile("s_mov_b32 m0, %2\n\ts_nop 0\n\tglobal_load_lds_dwordx4 %0, %1" ::"v"(voff), "s"(sbase), "s"(ldsdst) : "memory");
}
DI bf16x8 lds_rd128(lds_cptr p) { return *(LDSP const bf16x8*)p; }
DI s16x4 lds_tr(lds_cptr p) { return __builtin_bit_cast(s16x4, __builtin_amdgcn_ds_read_tr16_b64_v4i16((LDSP s16x4*)p)); }
DI bf16x8 lds_tr2(lds_cptr p) {
    s16x4 a = lds_tr(p), b = lds_tr(p + 256);
    s16x8 r = {a[0], a[1], a[2], a[3], b[0], b[1], b[2], b[3]};
    return __builtin_bit_cast(bf16x8, r);
}
#define WAIT_BAR(N) asm volatile("s_waitcnt vmcnt(" #N ") lgkmcnt(0)\n\ts_barrier" ::: "memory")
DI int swap23(int v) { return (v & ~12) | ((v & 4) << 1) | ((v & 8) >> 1); }

constexpr int AB_SLOT = 39168, AB_W = 32768, AB_AUX = 156672;
constexpr int AB_K = 0, AB_V = 65536, AB_ONES = 131072;
constexpr int AB_FS = AB_ONES + 5632, AB_FH = AB_FS + 1024, AB_FC = AB_FH + 64;
constexpr int AB_LAND = AB_FC + 16;
constexpr int AB_LANDS = AB_FC + 64, AB_LANDW = AB_FC + 96;
constexpr int AB_KVDONE = AB_FC + 112, AB_SEEN = AB_FC + 116, AB_TAG = AB_FC + 128;
constexpr int AB_RG = AB_FS + 17920;
constexpr int AB_FW = AB_FS + 1280;
constexpr int AB_LDS = AB_AUX + 4096;
static_assert(AB_LDS <= 163840 && AB_FW + 16384 <= AB_LDS && AB_RG + 256 <= AB_AUX && AB_FW % 256 == 0, "LDS");

DI float max16(const f32x16& s) {
    float a = fmaxf(fmaxf(s[0], s[1]), s[2]), b = fmaxf(fmaxf(s[3], s[4]), s[5]);
    a = fmaxf(fmaxf(a, s[6]), s[7]); b = fmaxf(fmaxf(b, s[8]), s[9]);
    a = fmaxf(fmaxf(a, s[10]), s[11]); b = fmaxf(fmaxf(b, s[12]), s[13]);
    a = fmaxf(fmaxf(a, s[14]), s[15]);
    float m = fmaxf(a, b);
    auto rr = __builtin_amdgcn_permlane32_swap(__float_as_uint(m), __float_as_uint(m), false, false);
    return fmaxf(__uint_as_float(rr[0]), __uint_as_float(rr[1]));
}
DI void exp_pack(const f32x16& s, bf16x8& p0, bf16x8& p1) {
    u32x4 a, b;
#pragma unroll
    for (int i = 0; i < 4; ++i) a[i] = cvtpk(__builtin_amdgcn_exp2f(s[2 * i]), __builtin_amdgcn_exp2f(s[2 * i + 1]));
#pragma unroll
    for (int i = 0; i < 4; ++i) b[i] = cvtpk(__builtin_amdgcn_exp2f(s[8 + 2 * i]), __builtin_amdgcn_exp2f(s[8 + 2 * i + 1]));
    p0 = __builtin_bit_cast(bf16x8, a); p1 = __builtin_bit_cast(bf16x8, b);
}
DI void st16_dev(unsigned voff, u32x4 d, void* sbase) { asm volatile("global_store_dwordx4 %0, %1, %2 sc1\n\ts_nop 1" ::"v"(voff), "v"(d), "s"(sbase) : "memory"); }
DI void glds16_dev(unsigned voff, const void* sbase, unsigned ldsdst) {
    asm volatile("s_mov_b32 m0, %2\n\ts_nop 0\n\tglobal_load_lds_dwordx4 %0, %1 sc1" ::"v"(voff), "s"(sbase), "s"(ldsdst) : "memory");
}
typedef unsigned long long u64;
DI u64 ld_dev(const void* p) { return __hip_atomic_load((const u64*)p, __ATOMIC_RELAXED, __HIP_MEMORY_SCOPE_AGENT); }
DI void st_dev(void* p, u64 v) { __hip_atomic_store((u64*)p, v, __ATOMIC_RELAXED, __HIP_MEMORY_SCOPE_AGENT); }
DI void wait_flags(const char* flag, bool want, u64 tag) {
#pragma unroll 1
    for (unsigned spin = 0; spin < (1u << 20); ++spin) {
        const u64 v = want ? ld_dev(flag) : tag;
        if (__all(v == tag)) break;
        __builtin_amdgcn_s_sleep(1);
    }
}

#define BARRIER() asm volatile("s_barrier" ::: "memory")
#define WAIT_V(N) asm volatile("s_waitcnt vmcnt(" #N ")" ::: "memory")

DI void wait_flags_slow(const char* flag, bool want, u64 tag) {
#pragma unroll 1
    for (unsigned spin = 0; spin < (1u << 18); ++spin) {
        const u64 v = want ? ld_dev(flag) : tag;
        if (__all(v == tag)) break;
        __builtin_amdgcn_s_sleep(16);
    }
}
DI void wait_flags_mid(const char* flag, bool want, u64 tag) {
#pragma unroll 1
    for (unsigned spin = 0; spin < (1u << 19); ++spin) {
        const u64 v = want ? ld_dev(flag) : tag;
        if (__all(v == tag)) break;
        __builtin_amdgcn_s_sleep(4);
    }
}
DI f32x4 mfma16(bf16x8 a, bf16x8 b, f32x4 c) { return __builtin_amdgcn_mfma_f32_16x16x32_bf16(a, b, c, 0, 0, 0); }
DI void lds_count(LDSP unsigned* c) { asm volatile("s_waitcnt vmcnt(0) lgkmcnt(0)" ::: "memory"); if ((threadIdx.x & 63) == 0) __hip_atomic_fetch_add(c, 1u, __ATOMIC_RELAXED, __HIP_MEMORY_SCOPE_WORKGROUP); }
DI void lds_count_l(LDSP unsigned* c) { asm volatile("s_waitcnt lgkmcnt(0)" ::: "memory"); if ((threadIdx.x & 63) == 0) __hip_atomic_fetch_add(c, 1u, __ATOMIC_RELAXED, __HIP_MEMORY_SCOPE_WORKGROUP); }
DI void lds_wait(LDSP unsigned* c, unsigned n) {
#pragma unroll 1
    for (unsigned spin = 0; spin < (1u << 20); ++spin) {
        if (__hip_atomic_load(c, __ATOMIC_RELAXED, __HIP_MEMORY_SCOPE_WORKGROUP) >= n) break;
        __builtin_amdgcn_s_sleep(1);
    }
    asm volatile("" ::: "memory");
}
DI void lds_wait_idle(LDSP unsigned* c, unsigned n) {
    __builtin_amdgcn_s_setprio(0);
#pragma unroll 1
    for (unsigned spin = 0; spin < (1u << 20); ++spin) {
        if (__hip_atomic_load(c, __ATOMIC_RELAXED, __HIP_MEMORY_SCOPE_WORKGROUP) >= n) break;
        __builtin_amdgcn_s_sleep(8);
    }
    asm volatile("" ::: "memory");
    __builtin_amdgcn_s_setprio(3);
}
DI void lds_wait_lo(LDSP unsigned* c, unsigned n) {
#pragma unroll 1
    for (unsigned spin = 0; spin < (1u << 18); ++spin) {
        if (__hip_atomic_load(c, __ATOMIC_RELAXED, __HIP_MEMORY_SCOPE_WORKGROUP) >= n) break;
        __builtin_amdgcn_s_sleep(4);
    }
    asm volatile("" ::: "memory");
}
DI unsigned top_key_dev(char* ws, int lane, u64 tag) {
    u64 v = 0;
#pragma unroll 1
    for (unsigned spin = 0; spin < (1u << 20); ++spin) {
        v = ld_dev(ws + WS_KEYS + lane * 8);
        if (__all((unsigned)(v >> 32) == (unsigned)tag)) break;
        __builtin_amdgcn_s_sleep(8);
    }
    unsigned key = (unsigned)v;
#pragma unroll
    for (int o = 32; o >= 1; o >>= 1) { const unsigned t = __shfl_xor(key, o); key = t > key ? t : key; }
    return __builtin_amdgcn_readfirstlane(key);
}
DI bf16x8 tr_frag(lds_cptr p, int step) { s16x4 a = lds_tr(p), c = lds_tr(p + step); s16x8 t = {a[0], a[1], a[2], a[3], c[0], c[1], c[2], c[3]}; return __builtin_bit_cast(bf16x8, t); }
DI void ffn_unit(char* lds, const u16* x, const u16* wg, const u16* wu, const u16* wd, char* ws, int u, int j, int lane, u64 tag) {
    const int n = lane & 15, g = lane >> 4, q = n >> 2, p = n & 3;
    const unsigned slot = lds_addr(lds) + AB_FW + j * 4096;
    lds_cptr sl = (lds_cptr)lds + AB_FW + j * 4096;
    bf16x8 ag[4], au[4], ad[8];
    const unsigned vo = (lane >> 1) * (FF * 2) + (lane & 1) * 16;
    const size_t wo = (size_t)(128 * j) * (FF * 2) + 32 * u;
#pragma unroll
    for (int i = 0; i < 4; ++i) glds16(vo, (const char*)wg + wo + (size_t)i * 32 * (FF * 2), slot + i * 1024);
    const unsigned key = top_key_dev(ws, lane, tag);
    const int idx = 0xFFFF - (key & 0xFFFF);
#pragma unroll
    for (int ks = 0; ks < 4; ++ks) ag[ks] = tr_frag(sl + (32 * ks + 8 * g + q) * 32 + p * 8, 128);
    asm volatile("s_waitcnt lgkmcnt(0)" : "+v"(ag[0]), "+v"(ag[1]), "+v"(ag[2]), "+v"(ag[3])::"memory");
#pragma unroll
    for (int i = 0; i < 4; ++i) glds16(vo, (const char*)wu + wo + (size_t)i * 32 * (FF * 2), slot + i * 1024);
    bf16x8 xs[4];
#pragma unroll
    for (int ks = 0; ks < 4; ++ks) xs[ks] = *(const bf16x8*)(x + idx * DM + 128 * j + 32 * ks + 8 * g);
    asm volatile("s_waitcnt vmcnt(0)" : "+v"(xs[0]), "+v"(xs[1]), "+v"(xs[2]), "+v"(xs[3])::"memory");
#pragma unroll
    for (int ks = 0; ks < 4; ++ks) au[ks] = tr_frag(sl + (32 * ks + 8 * g + q) * 32 + p * 8, 128);
    asm volatile("s_waitcnt lgkmcnt(0)" : "+v"(au[0]), "+v"(au[1]), "+v"(au[2]), "+v"(au[3])::"memory");
#pragma unroll
    for (int i = 0; i < 4; ++i) glds16((lane >> 4) * 1024 + (lane & 15) * 16, (const char*)wd + (size_t)(16 * u + 4 * i) * 1024 + j * 256, slot + i * 1024);
    f32x4 dg = {}, du = {};
#pragma unroll
    for (int ks = 0; ks < 4; ++ks) { dg = mfma16(ag[ks], xs[ks], dg); du = mfma16(au[ks], xs[ks], du); }
    LDSP float* fs = (LDSP float*)((lds_ptr)lds + AB_FS);
    LDSP unsigned* fc = (LDSP unsigned*)((lds_ptr)lds + AB_FC);
    if (n == 0) { *(LDSP f32x4*)(fs + j * 32 + 4 * g) = dg; *(LDSP f32x4*)(fs + j * 32 + 16 + 4 * g) = du; }
    lds_count_l(fc);
    if (j == 0) {
        lds_wait_lo(fc, 4);
        const int l32 = lane & 31;
        const float v = rbf((fs[l32] + fs[32 + l32]) + (fs[64 + l32] + fs[96 + l32]));
        const float up = __shfl(v, (lane & 15) + 16);
        const float hv = rbf(v / (1.f + __expf(-v))) * up;
        if (lane < 16) *(LDSP u16*)((lds_ptr)lds + AB_FH + lane * 2) = (u16)(cvtpk(hv, 0.f) & 0xFFFFu);
        lds_count_l(fc + 1);
    }
    asm volatile("s_waitcnt vmcnt(0)" ::: "memory");
#pragma unroll
    for (int t = 0; t < 8; ++t) ad[t] = tr_frag(sl + (8 * (g & 1) + q) * 256 + t * 32 + p * 8, 1024);
    lds_wait_lo(fc + 1, 1);
    u32x4 hb = *(LDSP const u32x4*)((lds_ptr)lds + AB_FH + (g & 1) * 16);
    if (g >= 2) hb = u32x4{0u, 0u, 0u, 0u};
    char* yb = ws + WS_YP + (size_t)u * 2048 + j * 512;
    f32x4 y[8];
#pragma unroll
    for (int t = 0; t < 8; ++t) y[t] = mfma16(ad[t], __builtin_bit_cast(bf16x8, hb), f32x4{});
    asm volatile("s_nop 7\n\ts_nop 1" : "+v"(y[0]), "+v"(y[1]), "+v"(y[2]), "+v"(y[3]), "+v"(y[4]), "+v"(y[5]), "+v"(y[6]), "+v"(y[7]));
    if (n == 0) {
#pragma unroll
        for (int t = 0; t < 8; ++t) st16_dev(t * 64 + g * 16, __builtin_bit_cast(u32x4, y[t]), yb);
    }

    lds_count(fc + 2);
    if (j == 0) { lds_wait_lo(fc + 2, 4); if (lane == 0) st_dev(ws + WS_FLAG_F + u * 128, tag); }
}
DI void ffn_reduce(char* lds, char* ws, int g8, int j, int lane, u64 tag) {
    const unsigned key = top_key_dev(ws, lane, tag);

#pragma unroll 1
    for (unsigned spin = 0; spin < (1u << 19); ++spin) {
        const u64 a = ld_dev(ws + WS_FLAG_F + lane * 128), b2 = ld_dev(ws + WS_FLAG_F + min(64 + lane, 85) * 128);
        if (__all(a == tag && b2 == tag)) break;
        __builtin_amdgcn_s_sleep(4);
    }

    const unsigned* yp = (const unsigned*)(ws + WS_YP) + 64 * g8 + lane;
    unsigned v[22];
#pragma unroll
    for (int i = 0; i < 22; ++i) { const int u = min(j + 4 * i, 85); v[i] = __hip_atomic_load(yp + u * DM, __ATOMIC_RELAXED, __HIP_MEMORY_SCOPE_AGENT); }
    float y = 0.f;
#pragma unroll
    for (int i = 0; i < 22; ++i) y += (j + 4 * i < 86) ? __uint_as_float(v[i]) : 0.f;
    LDSP float* fs = (LDSP float*)((lds_ptr)lds + AB_FS);
    LDSP unsigned* fc = (LDSP unsigned*)((lds_ptr)lds + AB_FC);

    fs[j * 64 + lane] = y;
    lds_count(fc);
    if (j == 0) {
        lds_wait_lo(fc, 4);
        const float ys = rbf((fs[lane] + fs[64 + lane]) + (fs[128 + lane] + fs[192 + lane]));
        const unsigned ob16 = key >> 16, rb = (ob16 & 0x8000u) ? (ob16 & 0x7FFFu) : (~ob16 & 0xFFFFu);
        const float rs = rbf(1.f / (1.f + __expf(-bf2f(rb))));
        __hip_atomic_store((unsigned*)(ws + WS_Y2) + 64 * g8 + lane, __float_as_uint(rbf(ys * rs)), __ATOMIC_RELAXED, __HIP_MEMORY_SCOPE_AGENT);
        if (lane == 0) __hip_atomic_store((unsigned*)(ws + WS_Y2) + 512 + g8, 0xFFFFu - (key & 0xFFFFu), __ATOMIC_RELAXED, __HIP_MEMORY_SCOPE_AGENT);
        asm volatile("s_waitcnt vmcnt(0)" ::: "memory");
        if (lane == 0) st_dev(ws + WS_FLAG_Y + g8 * 128, tag);
    }
}

DI void ab_loader(char* lds, const u16* x, const u16* wqkv, const u16* w_r, const u16* gate_w, const u16* wg, const u16* wu, const u16* wd, const u16* wo, char* ws, int head, int chunk, int j, int lane) {
    const unsigned lds0 = lds_addr(lds);
    const int rot = (head + chunk) & 7;
    __builtin_amdgcn_s_setprio(3);
    const unsigned xv0 = (lane >> 3) * 1024 + ((lane & 7) ^ ((lane >> 4) & 7)) * 16;
    const unsigned xv1 = (lane >> 3) * 1024 + ((lane & 7) ^ (((lane >> 4) + 4) & 7)) * 16;
    const char* xb = (const char*)x + (size_t)(256 * chunk + 64 * j) * 1024;
    auto issue_x = [&](int i) {
        const int s = (rot + i) & 7;
        const unsigned sl = lds0 + ((i + 1) % 3) * AB_SLOT;
#pragma unroll
        for (int q = 0; q < 8; ++q) glds16((q & 1) ? xv1 : xv0, xb + q * 8192 + s * 128, sl + (8 * j + q) * 1024);
    };

    issue_x(0);
    SBAR();
    unsigned wsrc[2];
#pragma unroll
    for (int e = 0; e < 2; ++e) {
        const int o = 768 * (2 * j + e) + 16 * lane;
        if (o < 4096) { const int cc = (o >> 4) & 3; wsrc[e] = (o >> 6) * 3072 + (cc < 2 ? 16 * cc : 1024 + 16 * (cc - 2)); }
        else { const int o2 = o - 4096, row = min(o2 >> 5, 63); wsrc[e] = row * 3072 + 2048 + 16 * ((o2 >> 4) & 1); }
    }
    const char* wb = (const char*)wqkv + head * 32;
    auto issue_w = [&](int i) {
        const int s = (rot + i) & 7;
        const unsigned sl = lds0 + ((i + 1) % 3) * AB_SLOT;
        if (lane < 48) { glds16(wsrc[0], wb + (size_t)s * 64 * 3072, sl + AB_W + 768 * (2 * j)); glds16(wsrc[1], wb + (size_t)s * 64 * 3072, sl + AB_W + 768 * (2 * j + 1)); }
    };
    auto issue = [&](int i) { issue_w(i); issue_x(i); };
    issue_w(0); issue(1);
    unsigned e_lo, e_hi;
    asm volatile("global_load_dword %0, %2, %3 sc1\n\tglobal_load_dword %1, %2, %3 offset:4 sc1" : "=&v"(e_lo), "=&v"(e_hi) : "v"(0u), "s"(ws + WS_EPOCH) : "memory");
    WAIT_BAR(12); issue(2);
    asm volatile("s_waitcnt vmcnt(10)\n\ts_barrier" : "+v"(e_lo), "+v"(e_hi)::"memory");
    const u64 tag = (((u64)__builtin_amdgcn_readfirstlane(e_hi) << 32) | __builtin_amdgcn_readfirstlane(e_lo)) + 1;
    if (j == 0 && lane == 0) *(LDSP u64*)((lds_ptr)lds + AB_TAG) = tag;
    issue(3);
    WAIT_BAR(10); issue(4);
    WAIT_BAR(10); issue(5);
    WAIT_BAR(10); issue(6);
    WAIT_BAR(10); issue(7);
    WAIT_BAR(10);
    WAIT_BAR(0);
    BARRIER();
    wait_flags(ws + WS_FLAG_A + (head * 64 + ((chunk + 1 + (lane >> 1)) & 7) * 8 + 2 * j + (lane & 1)) * 64, lane < 14, tag);

    {
        const char* kb = ws + WS_K + (size_t)head * 65536 + j * 2048;
        const char* vb = ws + WS_V + (size_t)head * 65536 + j * 2048;
        auto fetch = [&](int p) {
            const int cp = (chunk + p) & 7;
            glds16_dev(lane * 16, kb + cp * 8192, lds0 + AB_K + p * 8192 + j * 2048); glds16_dev(lane * 16, kb + cp * 8192 + 1024, lds0 + AB_K + p * 8192 + j * 2048 + 1024);
            glds16_dev(lane * 16, vb + cp * 8192, lds0 + AB_V + p * 8192 + j * 2048); glds16_dev(lane * 16, vb + cp * 8192 + 1024, lds0 + AB_V + p * 8192 + j * 2048 + 1024);
        };
        LDSP volatile unsigned* land = (LDSP volatile unsigned*)((lds_ptr)lds + AB_LAND) + j;
#define LANDED(N, p) do { WAIT_V(N); if (lane == 0) *land = p; } while (0)
        fetch(1); fetch(2);
        LANDED(4, 1);

        fetch(3); fetch(4); fetch(5); fetch(6); fetch(7);
        LANDED(20, 2); LANDED(16, 3); LANDED(12, 4); LANDED(8, 5); LANDED(4, 6); LANDED(0, 7);
#undef LANDED

    }
    __builtin_amdgcn_s_setprio(0);
    const int ce = blockIdx.x & 7, tp = 4 * ce + ((blockIdx.x >> 3) & 3), ct = blockIdx.x >> 5;
    const char* wb3 = (const char*)wo + (size_t)((j & 1) * 32) * 1024 + ct * 128 + (j >> 1) * 64;
    const unsigned wvo3 = (lane >> 2) * 1024 + (lane & 3) * 16;
    {
        u32x4 d0, d1, d2, d3;
        const unsigned pv = (128 * ((blockIdx.x >> 3) & 3) + 32 * j + (lane >> 3)) * 1024 + ct * 128 + (lane & 7) * 16;
        asm volatile("global_load_dwordx4 %0, %4, %8\n\tglobal_load_dwordx4 %1, %5, %8\n\tglobal_load_dwordx4 %2, %6, %8\n\tglobal_load_dwordx4 %3, %7, %8\n\ts_waitcnt vmcnt(0)"
                     : "=&v"(d0), "=&v"(d1), "=&v"(d2), "=&v"(d3) : "v"(pv), "v"(pv + 8192), "v"(pv + 16384), "v"(pv + 24576), "s"(wo) : "memory");
    }
    if (blockIdx.x < 94) {
        if (blockIdx.x < 86) { __builtin_amdgcn_s_setprio(2); ffn_unit(lds, x, wg, wu, wd, ws, blockIdx.x, j, lane, tag); }
        else { __builtin_amdgcn_s_setprio(3); ffn_reduce(lds, ws, blockIdx.x - 86, j, lane, tag); }
        __builtin_amdgcn_s_setprio(0);
    }

    if (blockIdx.x == 255 && j == 1) {
#pragma unroll
        for (int i = 0; i < 4; ++i) wait_flags_slow(ws + WS_FLAG_A + (64 * i + lane) * 512, true, tag);
        if (lane == 0) st_dev(ws + WS_EPOCH, tag);
    }
    if (j == 0) {
        wait_flags_mid(ws + WS_FLAG_Y + ct * 128, lane == 0, tag);
        if (lane == 0) *(LDSP volatile unsigned*)((lds_ptr)lds + AB_SEEN) = 1u;
    }
    lds_wait_idle((LDSP unsigned*)((lds_ptr)lds + AB_KVDONE), 8);

#pragma unroll
    for (int s = 0; s < 8; ++s)
#pragma unroll
        for (int q = 0; q < 2; ++q) glds16(wvo3, wb3 + (size_t)(64 * s + 16 * q) * 1024, lds0 + s * 16384 + 8192 + (2 * j + q) * 1024);
    {
        const char* fl = ws + WS_FLAG_O + ((4 * (j + 4 * ((lane >> 3) & 1)) + ((lane >> 1) & 3)) * 64 + 2 * tp + (lane & 1)) * 64;
        unsigned todo = 3;
        bool first = true;
#pragma unroll 1
        for (unsigned spin = 0; spin < (1u << 20) && todo; ++spin) {
            const u64 v = ld_dev(fl);
            const unsigned long long ok = __ballot(v == tag);
            if (first) { first = false; if (lane == 0) *((LDSP volatile unsigned*)((lds_ptr)lds + AB_LANDW) + j) = 1u; }
            const unsigned rdy = (((ok & 0xFFull) == 0xFFull ? 1u : 0u) | ((ok & 0xFF00ull) == 0xFF00ull ? 2u : 0u)) & todo;
            if (rdy) {
#pragma unroll
                for (int e = 0; e < 2; ++e) if (rdy & (1u << e)) {
                    const int s = j + 4 * e;
                    const char* ob = ws + WS_O + (size_t)(2 * tp) * 32768 + s * 4096;
#pragma unroll
                    for (int q = 0; q < 8; ++q) glds16_dev(lane * 16, ob + (q >> 2) * 32768 + (q & 3) * 1024, lds0 + s * 16384 + q * 1024);
                }
                WAIT_V(0);
                if (lane < 2 && (rdy & (1u << lane))) *((LDSP volatile unsigned*)((lds_ptr)lds + AB_LANDS) + j + 4 * lane) = 1u;
                todo &= ~rdy;
            } else __builtin_amdgcn_s_sleep(1);
        }
    }

}

__global__ __launch_bounds__(768, 3) void kab(const u16* __restrict__ x, const u16* __restrict__ wqkv, const u16* __restrict__ w_r, const u16* __restrict__ gate_w,
                                          const u16* __restrict__ wg, const u16* __restrict__ wu, const u16* __restrict__ wd, const u16* __restrict__ wo,
                                          char* __restrict__ ws, u16* __restrict__ out) {
    __shared__ __attribute__((aligned(16))) char lds[AB_LDS];
    const int tid = threadIdx.x, lane = tid & 63, w = __builtin_amdgcn_readfirstlane(tid >> 6);
    const int b = blockIdx.x, xcd = b & 7, loc = b >> 3;
    const int head = xcd * 4 + (loc >> 3), chunk = loc & 7;
    asm volatile("" ::"s"(x), "s"(wqkv), "s"(ws));
    if (__builtin_expect(w >= 8, 1)) { ab_loader(lds, x, wqkv, w_r, gate_w, wg, wu, wd, wo, ws, head, chunk, w - 8, lane); return; }
    const int r = lane & 31, h = lane >> 5;
    lds_cptr L = (lds_cptr)lds;
    const int rot = (head + chunk) & 7;
    unsigned xo[4];
#pragma unroll
    for (int ks = 0; ks < 4; ++ks) xo[ks] = (32 * w + r) * 128 + ((2 * ks + h) ^ ((r >> 1) & 7)) * 16;
    const int blk = (lane >> 4) & 1, q4 = (lane & 15) >> 2, p4 = lane & 3;
    const unsigned wq = AB_W + (8 * h + q4) * 64 + blk * 32 + (((p4 & 1) << 1) | (p4 >> 1)) * 8;
    const unsigned wva = blk ? (8 * h + q4) * 8 : (8 * h + q4) * 32 + p4 * 8, wvk = blk ? 128 : 512;
    f32x16 d1 = {}, d2 = {};
    bf16x8 xa[2], qa[2], va[2], xb2[2], qb[2], vb2[2];
    auto rd = [&](int i, int k0, bf16x8 (&xf)[2], bf16x8 (&fq)[2], bf16x8 (&fv)[2]) {
        const int s = (rot + i) & 7;
        lds_cptr sl = L + ((i + 1) % 3) * AB_SLOT;
        lds_cptr vv = L + wva + (blk ? AB_AUX + s * 512 : ((i + 1) % 3) * AB_SLOT + AB_W + 4096);
#pragma unroll
        for (int e = 0; e < 2; ++e) {
            const int ks = k0 + e;
            xf[e] = lds_rd128(sl + xo[ks]);
            fq[e] = lds_tr2(sl + wq + ks * 1024);
            s16x4 a = lds_tr(vv + ks * wvk), c = lds_tr(vv + ks * wvk + (wvk >> 2));
            s16x8 t = {a[0], a[1], a[2], a[3], c[0], c[1], c[2], c[3]};
            fv[e] = __builtin_bit_cast(bf16x8, t);
        }
        SBAR();
    };
    auto mm = [&](bf16x8 (&xf)[2], bf16x8 (&fq)[2], bf16x8 (&fv)[2]) {
#pragma unroll
        for (int e = 0; e < 2; ++e) { d1 = MFMA32(fq[e], xf[e], d1); d2 = MFMA32(xf[e], fv[e], d2); }
        SBAR();
    };
#define LBAR() asm volatile("s_waitcnt lgkmcnt(0)\n\ts_barrier" ::: "memory")
    { u32x4 ones = {0x3F803F80u, 0x3F803F80u, 0x3F803F80u, 0x3F803F80u}; if (tid < 352) *(LDSP u32x4*)((lds_ptr)lds + AB_ONES + tid * 16) = ones; }
    if (tid < 32) *(LDSP unsigned*)((lds_ptr)lds + AB_FC + tid * 4) = 0u;
    *(LDSP unsigned*)((lds_ptr)lds + AB_AUX + tid * 8) = (unsigned)w_r[tid] | ((unsigned)gate_w[tid] << 16);
    LBAR();
    rd(0, 0, xa, qa, va);
#define A_STAGE(i) rd(i, 2, xb2, qb, vb2); mm(xa, qa, va); if ((i) < 7) { LBAR(); rd((i) + 1, 0, xa, qa, va); } mm(xb2, qb, vb2);
    A_STAGE(0) A_STAGE(1) A_STAGE(2) A_STAGE(3) A_STAGE(4) A_STAGE(5) A_STAGE(6) A_STAGE(7)
#undef A_STAGE

    u32x4 qw, kw, vw0, vw1;
#pragma unroll
    for (int i = 0; i < 4; ++i) {
        kw[i] = cvtpk(d1[8 + 2 * i], d1[8 + 2 * i + 1]);
        vw0[i] = cvtpk(d2[2 * i], d2[2 * i + 1]); vw1[i] = cvtpk(d2[8 + 2 * i], d2[8 + 2 * i + 1]);
    }
    const bool isv = r < 16;
    const unsigned vpos = h * 256 + swap23(r & 15) * 16;
    {
        char* kg = ws + WS_K + (size_t)head * 65536 + (8 * chunk + w) * 1024;
        char* vg = ws + WS_V + (size_t)head * 65536 + (8 * chunk + w) * 1024;
        st16_dev(lane * 16, kw, kg);
        if (isv) { st16_dev(vpos, vw0, vg); st16_dev(vpos + 512, vw1, vg); }
    }
    *(LDSP u32x4*)((lds_ptr)lds + AB_K + w * 1024 + lane * 16) = kw;
    if (isv) { *(LDSP u32x4*)((lds_ptr)lds + AB_V + w * 1024 + vpos) = vw0; *(LDSP u32x4*)((lds_ptr)lds + AB_V + w * 1024 + vpos + 512) = vw1; }
#pragma unroll
    for (int i = 0; i < 4; ++i) qw[i] = cvtpk(d1[2 * i] * QSCALE, d1[2 * i + 1] * QSCALE);
    const bf16x8 qf = __builtin_bit_cast(bf16x8, qw);
    f32x16 zero = {};
    const float mref = max16(MFMA32(__builtin_bit_cast(bf16x8, kw), qf, zero));
    f32x16 cneg;
#pragma unroll
    for (int i = 0; i < 16; ++i) cneg[i] = -mref;

    const int tp = 4 * xcd + (loc & 3), ct = loc >> 2;
    const int tg = w & 3, cg = w >> 2;
    const int token = 64 * tp + 16 * tg + (lane & 15), col = 64 * ct + 32 * cg + 8 * (lane >> 4);
    const u32x4 xv = *(const u32x4*)(x + token * DM + col);
    LBAR();

    lds_cptr kp = L + AB_K + lane * 16;
    lds_cptr vp = isv ? L + AB_V + h * 256 + r * 16 : L + AB_ONES + ((r >= 20 && r < 28) ? 64 : 0);
    const unsigned vinc = isv ? 4096u : 0u;
    const u64 tag = *(LDSP const u64*)((lds_ptr)lds + AB_TAG);
    const bf16x8 kfirst = lds_rd128(kp);
    bf16x8 kf = lds_rd128(kp + 1024);
    bf16x8 v0 = lds_rd128(vp), v1 = lds_rd128(vp + 512);
    asm volatile("s_waitcnt vmcnt(0)" ::: "memory");
    if (lane == 0) st_dev(ws + WS_FLAG_A + (head * 64 + 8 * chunk + w) * 64, tag);
    f32x16 s0 = MFMA32(kfirst, qf, cneg);
    f32x16 o = {}, s1;
    u32x4 pa, pb = {0u, 0u, 0u, 0u};
#define EX(S, i) S[i] = __builtin_amdgcn_exp2f(S[i]); SBAR();
#define CV(P, S, i) P[i & 3] = cvtpk(S[2 * (i)], S[2 * (i) + 1]); SBAR();
#define STEP(SC, SN, u) \
        SN = MFMA32(kf, qf, cneg); SBAR(); \
        kf = lds_rd128(kq + ((u) + 2) * 1024); SBAR(); \
        EX(SC, 0) EX(SC, 1) EX(SC, 2) EX(SC, 3) \
        o = MFMA32(v1, __builtin_bit_cast(bf16x8, pb), o); SBAR(); \
        v1 = lds_rd128(vp + (u) * 1024 + 512); SBAR(); \
        CV(pa, SC, 0) EX(SC, 4) EX(SC, 5) CV(pa, SC, 1) EX(SC, 6) EX(SC, 7) CV(pa, SC, 2) EX(SC, 8) EX(SC, 9) CV(pa, SC, 3) EX(SC, 10) EX(SC, 11) \
        o = MFMA32(v0, __builtin_bit_cast(bf16x8, pa), o); SBAR(); \
        v0 = lds_rd128(vp + ((u) + 1) * 1024); SBAR(); \
        CV(pb, SC, 4) EX(SC, 12) EX(SC, 13) CV(pb, SC, 5) EX(SC, 14) EX(SC, 15) CV(pb, SC, 6) CV(pb, SC, 7)
    lds_cptr kq = kp;
    auto wait_words = [&](int off, unsigned p) -> unsigned {
        unsigned have = 0;
#pragma unroll 1
        for (unsigned spin = 0; spin < (1u << 20); ++spin) {
            const u32x4 v = *(LDSP volatile const u32x4*)((lds_ptr)lds + off);
            have = __builtin_amdgcn_readfirstlane(min(min(v[0], v[1]), min(v[2], v[3])));
            if (have >= p) break;
            __builtin_amdgcn_s_sleep(1);
        }
        asm volatile("" ::: "memory");
        return have;
    };
    unsigned kv_have = 0;
    auto landed = [&](unsigned p) { if (kv_have < p) kv_have = wait_words(AB_LAND, p); };
    if (w == head) {
        LDSP float* rg = (LDSP float*)((lds_ptr)lds + AB_RG);
        if ((r & 30) == 16) {
#pragma unroll
            for (int qd = 0; qd < 4; ++qd) *(LDSP f32x4*)(rg + (r & 1) * 32 + 8 * qd + 4 * h) = f32x4{d2[4 * qd], d2[4 * qd + 1], d2[4 * qd + 2], d2[4 * qd + 3]};
        }
        const int token = 256 * chunk + 32 * w + r;
        const unsigned rb = cvtpk(rg[r], 0.f) & 0xFFFFu;
        unsigned key = (((rb & 0x8000u) ? (~rb & 0xFFFFu) : (rb | 0x8000u)) << 16) | (0xFFFFu - token);
#pragma unroll
        for (int o = 16; o >= 1; o >>= 1) { const unsigned t = __shfl_xor(key, o); key = t > key ? t : key; }
        if (lane == 0) st_dev(ws + WS_KEYS + (8 * chunk + w) * 8, (tag << 32) | key);
        const float gs = rbf(rg[32 + r]);
        const float g = rbf(1.f / (1.f + __expf(-gs)));
        if (lane < 32) st_dev(ws + WS_GATE + token * 8, (tag << 32) | __float_as_uint(g));
    }

    constexpr int PRIO_MID = 5;
    if (w < 4) __builtin_amdgcn_s_setprio(0); else __builtin_amdgcn_s_setprio(1);
    const int prio_at = w < 4 ? PRIO_MID : -1;
    unsigned special = 0x2AAC;
#pragma unroll 1
    for (int m = 0; m < 16; ++m) {
        if (__builtin_expect((special >> m) & 1, 0)) {
            if (m == 2) {

                landed(1);

                s0 = MFMA32(lds_rd128(kq), qf, cneg); kf = lds_rd128(kq + 1024); v0 = lds_rd128(vp);
            } else if (m & 1) landed((m + 1) >> 1);
            if (kv_have >= 7) special = 0;
        }
        SBAR();
        STEP(s0, s1, 0) STEP(s1, s0, 1)
        asm volatile("s_cmp_eq_u32 %0, %1\n\ts_cbranch_scc0 1f\n\ts_setprio 2\n1:" ::"s"(m), "s"(prio_at) : "scc");
        SBAR();
        STEP(s0, s1, 2) STEP(s1, s0, 3)
        kq += 4096; vp += vinc;
    }
#undef STEP
#undef EX
#undef CV
    o = MFMA32(v1, __builtin_bit_cast(bf16x8, pb), o);

    float l = o[8];
    float chk = 0.f;
#pragma unroll
    for (int i = 0; i < 8; ++i) chk += fabsf(o[i]);
    const bool bad = !(l < 1.152921504606847e18f) || !(chk < 3.0e38f);
    if (__any(bad)) {
        float m = -INFINITY;
        o = f32x16{};
        lds_cptr vq = isv ? L + AB_V + h * 256 + r * 16 : L + AB_ONES;
        for (int t = 0; t < 64; ++t) {
            f32x16 s = MFMA32(lds_rd128(kp + t * 1024), qf, zero);
            const float mn = fmaxf(m, max16(s));
            const float f = __builtin_amdgcn_exp2f(m - mn);
            m = mn;
#pragma unroll
            for (int i = 0; i < 16; ++i) { s[i] -= mn; o[i] *= f; }
            bf16x8 p0, p1;
            exp_pack(s, p0, p1);
            lds_cptr vt = vq + (isv ? t * 1024 : 0);
            o = MFMA32(lds_rd128(vt), p0, o); o = MFMA32(lds_rd128(vt + 512), p1, o);
        }
        l = o[8];
    }
    const float inv = 1.f / l;
    u32x4 ov;
#pragma unroll
    for (int i = 0; i < 4; ++i) ov[i] = cvtpk(o[2 * i] * inv, o[2 * i + 1] * inv);
    st16_dev(lane * 16, ov, ws + WS_O + (size_t)((8 * chunk + w) * 32 + head) * 1024);
    asm volatile("s_waitcnt lgkmcnt(0)" ::: "memory");
    if (lane == 0) __hip_atomic_fetch_add((LDSP unsigned*)((lds_ptr)lds + AB_KVDONE), 1u, __ATOMIC_RELAXED, __HIP_MEMORY_SCOPE_WORKGROUP);
    __builtin_amdgcn_s_setprio(0);
    asm volatile("s_waitcnt vmcnt(0)" ::: "memory");
    if (lane == 0) st_dev(ws + WS_FLAG_O + (head * 64 + 8 * chunk + w) * 64, tag);

    const int g4 = lane >> 4;
    const unsigned wq3 = 8192 + cg * 4096 + (8 * g4 + q4) * 64 + p4 * 16;
    const unsigned oo3 = (tg >> 1) * 4096 + (g4 >> 1) * 1024 + (g4 & 1) * 512 + (16 * (tg & 1) + (lane & 15)) * 16;
    f32x4 ac0 = {}, ac1 = {};
    u32x4 y0, y1; unsigned idxb;
    lds_wait((LDSP unsigned*)((lds_ptr)lds + AB_SEEN), 1);
    asm volatile("global_load_dwordx4 %0, %3, %4 sc1\n\tglobal_load_dwordx4 %1, %3, %4 offset:16 sc1\n\tglobal_load_dword %2, %5, %4 offset:2048 sc1"
                 : "=&v"(y0), "=&v"(y1), "=&v"(idxb) : "v"(col * 4), "s"(ws + WS_Y2), "v"(ct * 4) : "memory");
    unsigned gtb = 0;
#pragma unroll 1
    for (unsigned spin = 0; spin < (1u << 20); ++spin) {
        const u64 gv = ld_dev(ws + WS_GATE + token * 8);
        gtb = (unsigned)gv;
        if (__all((unsigned)(gv >> 32) == (unsigned)tag)) break;
        __builtin_amdgcn_s_sleep(2);
    }
    asm volatile("" : "+v"(y0), "+v"(y1), "+v"(idxb));
    {
        LDSP volatile const unsigned* lw = (LDSP volatile const unsigned*)((lds_ptr)lds + AB_LANDS) + (lane < 12 ? lane : 0);
        unsigned have = 0;
        auto need = [&](unsigned bits) {
            if ((have & bits) == bits) return;
#pragma unroll 1
            for (unsigned spin = 0; spin < (1u << 20); ++spin) {
                have = (unsigned)__ballot(*lw != 0u);
                if ((have & bits) == bits) break;
                __builtin_amdgcn_s_sleep(1);
            }
            asm volatile("" ::: "memory");
        };
        bf16x8 fa[6], fb[6];
        auto rd = [&](int s, bf16x8 (&f)[6]) {
            lds_cptr sb = L + s * 16384;
#pragma unroll
            for (int k2 = 0; k2 < 2; ++k2) { f[3 * k2] = lds_rd128(sb + oo3 + k2 * 2048); f[3 * k2 + 1] = lds_tr2(sb + wq3 + k2 * 2048); f[3 * k2 + 2] = lds_tr2(sb + wq3 + k2 * 2048 + 8); }
            SBAR();
        };
        auto mm = [&](bf16x8 (&f)[6]) {
#pragma unroll
            for (int k2 = 0; k2 < 2; ++k2) { ac0 = mfma16(f[3 * k2 + 1], f[3 * k2], ac0); ac1 = mfma16(f[3 * k2 + 2], f[3 * k2], ac1); }
            SBAR();
        };
        need(0xF01); rd(0, fa);
        need(0x002); rd(1, fb); mm(fa);
        need(0x004); rd(2, fa); mm(fb);
        need(0x008); rd(3, fb); mm(fa);
        need(0x010); rd(4, fa); mm(fb);
        need(0x020); rd(5, fb); mm(fa);
        need(0x040); rd(6, fa); mm(fb);
        need(0x080); rd(7, fb); mm(fa); mm(fb);
    }

    asm volatile("s_waitcnt vmcnt(0)" : "+v"(y0), "+v"(y1), "+v"(idxb)::"memory");
    {
        const bool sel = token == (int)idxb;
        const float gt = __uint_as_float(gtb), omg = 1.f - gt;
        u32x4 res;
#pragma unroll
        for (int i = 0; i < 4; ++i) {
            float o2[2];
#pragma unroll
            for (int e = 0; e < 2; ++e) {
                const int c = 2 * i + e;
                const float a = c < 4 ? ac0[c & 3] : ac1[c & 3];
                const float xx = e ? __uint_as_float(xv[i] & 0xFFFF0000u) : __uint_as_float(xv[i] << 16);
                const float ys = __uint_as_float(c < 4 ? y0[c & 3] : y1[c & 3]);
                o2[e] = gt * a + omg * (xx + (sel ? ys : 0.f));
            }
            res[i] = cvtpk(o2[0], o2[1]);
        }
        st16_dev((token * DM + col) * 2, res, out);
    }

}

extern "C" void kernel_launch(void* const* d_in, const int* in_sizes, int n_in, void* d_out, int out_size, void* d_ws, size_t ws_size, hipStream_t stream) {
    const u16* x = (const u16*)d_in[0];
    const u16* wqkv = (const u16*)d_in[1];
    const u16* wo = (const u16*)d_in[2];
    const u16* w_r = (const u16*)d_in[4];
    const u16* wg = (const u16*)d_in[5];
    const u16* wu = (const u16*)d_in[6];
    const u16* wd = (const u16*)d_in[7];
    const u16* gate_w = (const u16*)d_in[8];
    char* ws = (char*)d_ws;
    kab<<<256, 768, 0, stream>>>(x, wqkv, w_r, gate_w, wg, wu, wd, wo, ws, (u16*)d_out);
}
```

```cpp
#include <hip/hip_runtime.h>
#include <hip/hip_bf16.h>
#include <stdint.h>

typedef __bf16 bf16x8 __attribute__((ext_vector_type(8)));
typedef __bf16 bf16x2 __attribute__((ext_vector_type(2)));
typedef short s16x4 __attribute__((ext_vector_type(4)));
typedef short s16x8 __attribute__((ext_vector_type(8)));
typedef float f32x16 __attribute__((ext_vector_type(16)));
typedef float f32x4 __attribute__((ext_vector_type(4)));
typedef float f32x2 __attribute__((ext_vector_type(2)));
typedef unsigned u32x4 __attribute__((ext_vector_type(4)));
typedef unsigned short u16;
#define LDSP __attribute__((address_space(3)))
typedef LDSP const char* lds_cptr;
typedef LDSP char* lds_ptr;
#define DI __device__ __forceinline__
#define MFMA32(a, b, c) __builtin_amdgcn_mfma_f32_32x32x16_bf16(a, b, c, 0, 0, 0)
#define SBAR() __builtin_amdgcn_sched_barrier(0)

constexpr int DM = 512, FF = 1376;
constexpr float QSCALE = 0.25f * 1.4426950408889634f;

constexpr size_t WS_K = 2u << 20;
constexpr size_t WS_V = 4u << 20;
constexpr size_t WS_O = 6u << 20;
constexpr size_t WS_GATE = 8u << 20;
constexpr size_t WS_KEYS = WS_GATE + 16384;
constexpr size_t WS_YP = WS_KEYS + 9216;
constexpr size_t WS_EPOCH = 9u << 20;
constexpr size_t WS_FLAG_F = WS_EPOCH + 4096;
constexpr size_t WS_Y2 = WS_FLAG_F + 16384;
constexpr size_t WS_FLAG_Y = WS_Y2 + 4096;
constexpr size_t WS_FLAG_A = 10u << 20;
constexpr size_t WS_FLAG_O = WS_FLAG_A + 131072;

DI unsigned cvtpk(float lo, float hi) { f32x2 v = {lo, hi}; bf16x2 b = __builtin_convertvector(v, bf16x2); return __builtin_bit_cast(unsigned, b); }
DI float bf2f(unsigned b) { return __uint_as_float(b << 16); }
DI float rbf(float x) { return __uint_as_float(cvtpk(x, 0.f) << 16); }
DI unsigned lds_addr(const void* p) { return (unsigned)(size_t)(LDSP const char*)p; }
DI void glds16(unsigned voff, const void* sbase, unsigned ldsdst) {
    asm volatile("s_mov_b32 m0, %2\n\ts_nop 0\n\tglobal_load_lds_dwordx4 %0, %1" ::"v"(voff), "s"(sbase), "s"(ldsdst) : "memory");
}
DI bf16x8 lds_rd128(lds_cptr p) { return *(LDSP const bf16x8*)p; }
DI s16x4 lds_tr(lds_cptr p) { return __builtin_bit_cast(s16x4, __builtin_amdgcn_ds_read_tr16_b64_v4i16((LDSP s16x4*)p)); }
DI bf16x8 lds_tr2(lds_cptr p) {
    s16x4 a = lds_tr(p), b = lds_tr(p + 256);
    s16x8 r = {a[0], a[1], a[2], a[3], b[0], b[1], b[2], b[3]};
    return __builtin_bit_cast(bf16x8, r);
}
#define WAIT_BAR(N) asm volatile("s_waitcnt vmcnt(" #N ") lgkmcnt(0)\n\ts_barrier" ::: "memory")
template <bool B> struct BoolC { static constexpr bool value = B; };
DI int swap23(int v) { return (v & ~12) | ((v & 4) << 1) | ((v & 8) >> 1); }

constexpr int AB_SLOT = 39168, AB_W = 32768, AB_AUX = 156672;
constexpr int AB_K = 0, AB_V = 65536, AB_ONES = 131072;
constexpr int AB_FS = AB_ONES + 5632, AB_FH = AB_FS + 1024, AB_FC = AB_FH + 64;
constexpr int AB_LAND = AB_FC + 16;
constexpr int AB_LANDS = AB_FC + 64, AB_LANDW = AB_FC + 96;
constexpr int AB_KVDONE = AB_FC + 112, AB_SEEN = AB_FC + 116, AB_TAG = AB_FC + 128;
constexpr int AB_RG = AB_FS + 17920;
constexpr int AB_FW = AB_FS + 1280;
constexpr int AB_LDS = AB_AUX + 4096;
static_assert(AB_LDS <= 163840 && AB_FW + 16384 <= AB_LDS && AB_RG + 256 <= AB_AUX && AB_FW % 256 == 0, "LDS");

DI float max16(const f32x16& s) {
    float a = fmaxf(fmaxf(s[0], s[1]), s[2]), b = fmaxf(fmaxf(s[3], s[4]), s[5]);
    a = fmaxf(fmaxf(a, s[6]), s[7]); b = fmaxf(fmaxf(b, s[8]), s[9]);
    a = fmaxf(fmaxf(a, s[10]), s[11]); b = fmaxf(fmaxf(b, s[12]), s[13]);
    a = fmaxf(fmaxf(a, s[14]), s[15]);
    float m = fmaxf(a, b);
    auto rr = __builtin_amdgcn_permlane32_swap(__float_as_uint(m), __float_as_uint(m), false, false);
    return fmaxf(__uint_as_float(rr[0]), __uint_as_float(rr[1]));
}
DI void exp_pack(const f32x16& s, bf16x8& p0, bf16x8& p1) {
    u32x4 a, b;
#pragma unroll
    for (int i = 0; i < 4; ++i) a[i] = cvtpk(__builtin_amdgcn_exp2f(s[2 * i]), __builtin_amdgcn_exp2f(s[2 * i + 1]));
#pragma unroll
    for (int i = 0; i < 4; ++i) b[i] = cvtpk(__builtin_amdgcn_exp2f(s[8 + 2 * i]), __builtin_amdgcn_exp2f(s[8 + 2 * i + 1]));
    p0 = __builtin_bit_cast(bf16x8, a); p1 = __builtin_bit_cast(bf16x8, b);
}
DI void st16_dev(unsigned voff, u32x4 d, void* sbase) { asm volatile("global_store_dwordx4 %0, %1, %2 sc1\n\ts_nop 1" ::"v"(voff), "v"(d), "s"(sbase) : "memory"); }
DI void glds16_dev(unsigned voff, const void* sbase, unsigned ldsdst) {
    asm volatile("s_mov_b32 m0, %2\n\ts_nop 0\n\tglobal_load_lds_dwordx4 %0, %1 sc1" ::"v"(voff), "s"(sbase), "s"(ldsdst) : "memory");
}
typedef unsigned long long u64;
DI u64 ld_dev(const void* p) { return __hip_atomic_load((const u64*)p, __ATOMIC_RELAXED, __HIP_MEMORY_SCOPE_AGENT); }
DI void st_dev(void* p, u64 v) { __hip_atomic_store((u64*)p, v, __ATOMIC_RELAXED, __HIP_MEMORY_SCOPE_AGENT); }
DI void wait_flags(const char* flag, bool want, u64 tag) {
#pragma unroll 1
    for (unsigned spin = 0; spin < (1u << 20); ++spin) {
        const u64 v = want ? ld_dev(flag) : tag;
        if (__all(v == tag)) break;
        __builtin_amdgcn_s_sleep(1);
    }
}

#define BARRIER() asm volatile("s_barrier" ::: "memory")
#define WAIT_V(N) asm volatile("s_waitcnt vmcnt(" #N ")" ::: "memory")

DI void wait_flags_slow(const char* flag, bool want, u64 tag) {
#pragma unroll 1
    for (unsigned spin = 0; spin < (1u << 18); ++spin) {
        const u64 v = want ? ld_dev(flag) : tag;
        if (__all(v == tag)) break;
        __builtin_amdgcn_s_sleep(16);
    }
}
DI void wait_flags_mid(const char* flag, bool want, u64 tag) {
#pragma unroll 1
    for (unsigned spin = 0; spin < (1u << 19); ++spin) {
        const u64 v = want ? ld_dev(flag) : tag;
        if (__all(v == tag)) break;
        __builtin_amdgcn_s_sleep(4);
    }
}
DI f32x4 mfma16(bf16x8 a, bf16x8 b, f32x4 c) { return __builtin_amdgcn_mfma_f32_16x16x32_bf16(a, b, c, 0, 0, 0); }
DI void lds_count(LDSP unsigned* c) { asm volatile("s_waitcnt vmcnt(0) lgkmcnt(0)" ::: "memory"); if ((threadIdx.x & 63) == 0) __hip_atomic_fetch_add(c, 1u, __ATOMIC_RELAXED, __HIP_MEMORY_SCOPE_WORKGROUP); }
DI void lds_count_l(LDSP unsigned* c) { asm volatile("s_waitcnt lgkmcnt(0)" ::: "memory"); if ((threadIdx.x & 63) == 0) __hip_atomic_fetch_add(c, 1u, __ATOMIC_RELAXED, __HIP_MEMORY_SCOPE_WORKGROUP); }
DI void lds_wait(LDSP unsigned* c, unsigned n) {
#pragma unroll 1
    for (unsigned spin = 0; spin < (1u << 20); ++spin) {
        if (__hip_atomic_load(c, __ATOMIC_RELAXED, __HIP_MEMORY_SCOPE_WORKGROUP) >= n) break;
        __builtin_amdgcn_s_sleep(1);
    }
    asm volatile("" ::: "memory");
}
DI void lds_wait_idle(LDSP unsigned* c, unsigned n) {
    __builtin_amdgcn_s_setprio(0);
#pragma unroll 1
    for (unsigned spin = 0; spin < (1u << 20); ++spin) {
        if (__hip_atomic_load(c, __ATOMIC_RELAXED, __HIP_MEMORY_SCOPE_WORKGROUP) >= n) break;
        __builtin_amdgcn_s_sleep(8);
    }
    asm volatile("" ::: "memory");
    __builtin_amdgcn_s_setprio(3);
}
DI void lds_wait_lo(LDSP unsigned* c, unsigned n) {
#pragma unroll 1
    for (unsigned spin = 0; spin < (1u << 18); ++spin) {
        if (__hip_atomic_load(c, __ATOMIC_RELAXED, __HIP_MEMORY_SCOPE_WORKGROUP) >= n) break;
        __builtin_amdgcn_s_sleep(4);
    }
    asm volatile("" ::: "memory");
}
DI unsigned top_key_dev(char* ws, int lane, u64 tag) {
    u64 v = 0;
#pragma unroll 1
    for (unsigned spin = 0; spin < (1u << 20); ++spin) {
        v = ld_dev(ws + WS_KEYS + lane * 8);
        if (__all((unsigned)(v >> 32) == (unsigned)tag)) break;
        __builtin_amdgcn_s_sleep(8);
    }
    unsigned key = (unsigned)v;
#pragma unroll
    for (int o = 32; o >= 1; o >>= 1) { const unsigned t = __shfl_xor(key, o); key = t > key ? t : key; }
    return __builtin_amdgcn_readfirstlane(key);
}
DI bf16x8 tr_frag(lds_cptr p, int step) { s16x4 a = lds_tr(p), c = lds_tr(p + step); s16x8 t = {a[0], a[1], a[2], a[3], c[0], c[1], c[2], c[3]}; return __builtin_bit_cast(bf16x8, t); }
DI void ffn_unit(char* lds, const u16* x, const u16* wg, const u16* wu, const u16* wd, char* ws, int u, int j, int lane, u64 tag) {
    const int n = lane & 15, g = lane >> 4, q = n >> 2, p = n & 3;
    const unsigned slot = lds_addr(lds) + AB_FW + j * 4096;
    lds_cptr sl = (lds_cptr)lds + AB_FW + j * 4096;
    bf16x8 ag[4], au[4], ad[8];
    const unsigned vo = (lane >> 1) * (FF * 2) + (lane & 1) * 16;
    const size_t wo = (size_t)(128 * j) * (FF * 2) + 32 * u;
#pragma unroll
    for (int i = 0; i < 4; ++i) glds16(vo, (const char*)wg + wo + (size_t)i * 32 * (FF * 2), slot + i * 1024);
    const unsigned key = top_key_dev(ws, lane, tag);
    const int idx = 0xFFFF - (key & 0xFFFF);
#pragma unroll
    for (int ks = 0; ks < 4; ++ks) ag[ks] = tr_frag(sl + (32 * ks + 8 * g + q) * 32 + p * 8, 128);
    asm volatile("s_waitcnt lgkmcnt(0)" : "+v"(ag[0]), "+v"(ag[1]), "+v"(ag[2]), "+v"(ag[3])::"memory");
#pragma unroll
    for (int i = 0; i < 4; ++i) glds16(vo, (const char*)wu + wo + (size_t)i * 32 * (FF * 2), slot + i * 1024);
    bf16x8 xs[4];
#pragma unroll
    for (int ks = 0; ks < 4; ++ks) xs[ks] = *(const bf16x8*)(x + idx * DM + 128 * j + 32 * ks + 8 * g);
    asm volatile("s_waitcnt vmcnt(0)" : "+v"(xs[0]), "+v"(xs[1]), "+v"(xs[2]), "+v"(xs[3])::"memory");
#pragma unroll
    for (int ks = 0; ks < 4; ++ks) au[ks] = tr_frag(sl + (32 * ks + 8 * g + q) * 32 + p * 8, 128);
    asm volatile("s_waitcnt lgkmcnt(0)" : "+v"(au[0]), "+v"(au[1]), "+v"(au[2]), "+v"(au[3])::"memory");
#pragma unroll
    for (int i = 0; i < 4; ++i) glds16((lane >> 4) * 1024 + (lane & 15) * 16, (const char*)wd + (size_t)(16 * u + 4 * i) * 1024 + j * 256, slot + i * 1024);
    f32x4 dg = {}, du = {};
#pragma unroll
    for (int ks = 0; ks < 4; ++ks) { dg = mfma16(ag[ks], xs[ks], dg); du = mfma16(au[ks], xs[ks], du); }
    LDSP float* fs = (LDSP float*)((lds_ptr)lds + AB_FS);
    LDSP unsigned* fc = (LDSP unsigned*)((lds_ptr)lds + AB_FC);
    if (n == 0) { *(LDSP f32x4*)(fs + j * 32 + 4 * g) = dg; *(LDSP f32x4*)(fs + j * 32 + 16 + 4 * g) = du; }
    lds_count_l(fc);
    if (j == 0) {
        lds_wait_lo(fc, 4);
        const int l32 = lane & 31;
        const float v = rbf((fs[l32] + fs[32 + l32]) + (fs[64 + l32] + fs[96 + l32]));
        const float up = __shfl(v, (lane & 15) + 16);
        const float hv = rbf(v / (1.f + __expf(-v))) * up;
        if (lane < 16) *(LDSP u16*)((lds_ptr)lds + AB_FH + lane * 2) = (u16)(cvtpk(hv, 0.f) & 0xFFFFu);
        lds_count_l(fc + 1);
    }
    asm volatile("s_waitcnt vmcnt(0)" ::: "memory");
#pragma unroll
    for (int t = 0; t < 8; ++t) ad[t] = tr_frag(sl + (8 * (g & 1) + q) * 256 + t * 32 + p * 8, 1024);
    lds_wait_lo(fc + 1, 1);
    u32x4 hb = *(LDSP const u32x4*)((lds_ptr)lds + AB_FH + (g & 1) * 16);
    if (g >= 2) hb = u32x4{0u, 0u, 0u, 0u};
    char* yb = ws + WS_YP + (size_t)u * 2048 + j * 512;
    f32x4 y[8];
#pragma unroll
    for (int t = 0; t < 8; ++t) y[t] = mfma16(ad[t], __builtin_bit_cast(bf16x8, hb), f32x4{});
    asm volatile("s_nop 7\n\ts_nop 1" : "+v"(y[0]), "+v"(y[1]), "+v"(y[2]), "+v"(y[3]), "+v"(y[4]), "+v"(y[5]), "+v"(y[6]), "+v"(y[7]));
    if (n == 0) {
#pragma unroll
        for (int t = 0; t < 8; ++t) st16_dev(t * 64 + g * 16, __builtin_bit_cast(u32x4, y[t]), yb);
    }

    lds_count(fc + 2);
    if (j == 0) { lds_wait_lo(fc + 2, 4); if (lane == 0) st_dev(ws + WS_FLAG_F + u * 128, tag); }
}
DI void ffn_reduce(char* lds, char* ws, int g8, int j, int lane, u64 tag) {
    const unsigned key = top_key_dev(ws, lane, tag);

#pragma unroll 1
    for (unsigned spin = 0; spin < (1u << 19); ++spin) {
        const u64 a = ld_dev(ws + WS_FLAG_F + lane * 128), b2 = ld_dev(ws + WS_FLAG_F + min(64 + lane, 85) * 128);
        if (__all(a == tag && b2 == tag)) break;
        __builtin_amdgcn_s_sleep(4);
    }

    const unsigned* yp = (const unsigned*)(ws + WS_YP) + 64 * g8 + lane;
    unsigned v[22];
#pragma unroll
    for (int i = 0; i < 22; ++i) { const int u = min(j + 4 * i, 85); v[i] = __hip_atomic_load(yp + u * DM, __ATOMIC_RELAXED, __HIP_MEMORY_SCOPE_AGENT); }
    float y = 0.f;
#pragma unroll
    for (int i = 0; i < 22; ++i) y += (j + 4 * i < 86) ? __uint_as_float(v[i]) : 0.f;
    LDSP float* fs = (LDSP float*)((lds_ptr)lds + AB_FS);
    LDSP unsigned* fc = (LDSP unsigned*)((lds_ptr)lds + AB_FC);

    fs[j * 64 + lane] = y;
    lds_count(fc);
    if (j == 0) {
        lds_wait_lo(fc, 4);
        const float ys = rbf((fs[lane] + fs[64 + lane]) + (fs[128 + lane] + fs[192 + lane]));
        const unsigned ob16 = key >> 16, rb = (ob16 & 0x8000u) ? (ob16 & 0x7FFFu) : (~ob16 & 0xFFFFu);
        const float rs = rbf(1.f / (1.f + __expf(-bf2f(rb))));
        __hip_atomic_store((unsigned*)(ws + WS_Y2) + 64 * g8 + lane, __float_as_uint(rbf(ys * rs)), __ATOMIC_RELAXED, __HIP_MEMORY_SCOPE_AGENT);
        if (lane == 0) __hip_atomic_store((unsigned*)(ws + WS_Y2) + 512 + g8, 0xFFFFu - (key & 0xFFFFu), __ATOMIC_RELAXED, __HIP_MEMORY_SCOPE_AGENT);
        asm volatile("s_waitcnt vmcnt(0)" ::: "memory");
        if (lane == 0) st_dev(ws + WS_FLAG_Y + g8 * 128, tag);
    }
}

DI void ab_loader(char* lds, const u16* x, const u16* wqkv, const u16* w_r, const u16* gate_w, const u16* wg, const u16* wu, const u16* wd, const u16* wo, char* ws, int head, int chunk, int j, int lane) {
    const unsigned lds0 = lds_addr(lds);
    const int rot = head & 7;
    __builtin_amdgcn_s_setprio(3);
    const unsigned xv0 = (lane >> 3) * 1024 + ((lane & 7) ^ ((lane >> 4) & 7)) * 16;
    const unsigned xv1 = (lane >> 3) * 1024 + ((lane & 7) ^ (((lane >> 4) + 4) & 7)) * 16;
    unsigned wsrc[2];
#pragma unroll
    for (int e = 0; e < 2; ++e) {
        const int o = min(768 * (2 * j + e) + 16 * lane, 6128), m = o >> 11, pp = (o >> 4) & 127;
        const int row = (pp >> 1) ^ (((pp >> 4) & 1) << 2);
        wsrc[e] = row * 3072 + m * 1024 + (pp & 1) * 16;
    }
    const char* xb = (const char*)x + (size_t)(256 * chunk + 64 * j) * 1024;
    const char* wb = (const char*)wqkv + head * 32;
    auto issue = [&](int i) {
        const int s = (rot + i) & 7;
        const unsigned sl = lds0 + ((i + 1) % 3) * AB_SLOT;
        if (lane < 48) { glds16(wsrc[0], wb + (size_t)s * 64 * 3072, sl + AB_W + 768 * (2 * j)); glds16(wsrc[1], wb + (size_t)s * 64 * 3072, sl + AB_W + 768 * (2 * j + 1)); }
#pragma unroll
        for (int q = 0; q < 8; ++q) glds16((q & 1) ? xv1 : xv0, xb + q * 8192 + s * 128, sl + (8 * j + q) * 1024);
    };
    issue(0); issue(1);
    unsigned e_lo, e_hi;
    asm volatile("global_load_dword %0, %2, %3 sc1\n\tglobal_load_dword %1, %2, %3 offset:4 sc1" : "=&v"(e_lo), "=&v"(e_hi) : "v"(0u), "s"(ws + WS_EPOCH) : "memory");
    WAIT_BAR(12); issue(2);
    asm volatile("s_waitcnt vmcnt(10)\n\ts_barrier" : "+v"(e_lo), "+v"(e_hi)::"memory");
    const u64 tag = (((u64)__builtin_amdgcn_readfirstlane(e_hi) << 32) | __builtin_amdgcn_readfirstlane(e_lo)) + 1;
    if (j == 0 && lane == 0) *(LDSP u64*)((lds_ptr)lds + AB_TAG) = tag;
    issue(3);
    WAIT_BAR(10); issue(4);
    WAIT_BAR(10); issue(5);
    WAIT_BAR(10); issue(6);
    WAIT_BAR(10); issue(7);
    WAIT_BAR(10);
    WAIT_BAR(0);
    BARRIER();
    wait_flags(ws + WS_FLAG_A + (head * 64 + ((chunk + 1 + (lane >> 1)) & 7) * 8 + 2 * j + (lane & 1)) * 64, lane < 14, tag);

    {
        const char* kb = ws + WS_K + (size_t)head * 65536 + j * 2048;
        const char* vb = ws + WS_V + (size_t)head * 65536 + j * 2048;
        auto fetch = [&](int p) {
            const int cp = (chunk + p) & 7;
            glds16_dev(lane * 16, kb + cp * 8192, lds0 + AB_K + p * 8192 + j * 2048); glds16_dev(lane * 16, kb + cp * 8192 + 1024, lds0 + AB_K + p * 8192 + j * 2048 + 1024);
            glds16_dev(lane * 16, vb + cp * 8192, lds0 + AB_V + p * 8192 + j * 2048); glds16_dev(lane * 16, vb + cp * 8192 + 1024, lds0 + AB_V + p * 8192 + j * 2048 + 1024);
        };
        LDSP volatile unsigned* land = (LDSP volatile unsigned*)((lds_ptr)lds + AB_LAND) + j;
#define LANDED(N, p) do { WAIT_V(N); if (lane == 0) *land = p; } while (0)
        fetch(1); fetch(2);
        LANDED(4, 1);

        fetch(3); fetch(4); fetch(5); fetch(6); fetch(7);
        LANDED(20, 2); LANDED(16, 3); LANDED(12, 4); LANDED(8, 5); LANDED(4, 6); LANDED(0, 7);
#undef LANDED

    }
    __builtin_amdgcn_s_setprio(0);
    const int ce = blockIdx.x & 7, tp = 4 * ce + ((blockIdx.x >> 3) & 3), ct = blockIdx.x >> 5;
    const char* wb3 = (const char*)wo + (size_t)((j & 1) * 32) * 1024 + ct * 128 + (j >> 1) * 64;
    const unsigned wvo3 = (lane >> 2) * 1024 + (lane & 3) * 16;
    {
        u32x4 d0, d1, d2, d3;
        const unsigned pv = (128 * ((blockIdx.x >> 3) & 3) + 32 * j + (lane >> 3)) * 1024 + ct * 128 + (lane & 7) * 16;
        asm volatile("global_load_dwordx4 %0, %4, %8\n\tglobal_load_dwordx4 %1, %5, %8\n\tglobal_load_dwordx4 %2, %6, %8\n\tglobal_load_dwordx4 %3, %7, %8\n\ts_waitcnt vmcnt(0)"
                     : "=&v"(d0), "=&v"(d1), "=&v"(d2), "=&v"(d3) : "v"(pv), "v"(pv + 8192), "v"(pv + 16384), "v"(pv + 24576), "s"(wo) : "memory");
    }
    if (blockIdx.x < 94) {
        if (blockIdx.x < 86) { __builtin_amdgcn_s_setprio(2); ffn_unit(lds, x, wg, wu, wd, ws, blockIdx.x, j, lane, tag); }
        else { __builtin_amdgcn_s_setprio(3); ffn_reduce(lds, ws, blockIdx.x - 86, j, lane, tag); }
        __builtin_amdgcn_s_setprio(0);
    }

    if (blockIdx.x == 255 && j == 1) {
#pragma unroll
        for (int i = 0; i < 4; ++i) wait_flags_slow(ws + WS_FLAG_A + (64 * i + lane) * 512, true, tag);
        if (lane == 0) st_dev(ws + WS_EPOCH, tag);
    }
    if (j == 0) {
        wait_flags_mid(ws + WS_FLAG_Y + ct * 128, lane == 0, tag);
        if (lane == 0) *(LDSP volatile unsigned*)((lds_ptr)lds + AB_SEEN) = 1u;
    }
    lds_wait_idle((LDSP unsigned*)((lds_ptr)lds + AB_KVDONE), 8);

#pragma unroll
    for (int s = 0; s < 8; ++s)
#pragma unroll
        for (int q = 0; q < 2; ++q) glds16(wvo3, wb3 + (size_t)(64 * s + 16 * q) * 1024, lds0 + s * 16384 + 8192 + (2 * j + q) * 1024);
    {
        const char* fl = ws + WS_FLAG_O + ((4 * (j + 4 * ((lane >> 3) & 1)) + ((lane >> 1) & 3)) * 64 + 2 * tp + (lane & 1)) * 64;
        unsigned todo = 3;
        bool first = true;
#pragma unroll 1
        for (unsigned spin = 0; spin < (1u << 20) && todo; ++spin) {
            const u64 v = ld_dev(fl);
            const unsigned long long ok = __ballot(v == tag);
            if (first) { first = false; if (lane == 0) *((LDSP volatile unsigned*)((lds_ptr)lds + AB_LANDW) + j) = 1u; }
            const unsigned rdy = (((ok & 0xFFull) == 0xFFull ? 1u : 0u) | ((ok & 0xFF00ull) == 0xFF00ull ? 2u : 0u)) & todo;
            if (rdy) {
#pragma unroll
                for (int e = 0; e < 2; ++e) if (rdy & (1u << e)) {
                    const int s = j + 4 * e;
                    const char* ob = ws + WS_O + (size_t)(2 * tp) * 32768 + s * 4096;
#pragma unroll
                    for (int q = 0; q < 8; ++q) glds16_dev(lane * 16, ob + (q >> 2) * 32768 + (q & 3) * 1024, lds0 + s * 16384 + q * 1024);
                }
                WAIT_V(0);
                if (lane < 2 && (rdy & (1u << lane))) *((LDSP volatile unsigned*)((lds_ptr)lds + AB_LANDS) + j + 4 * lane) = 1u;
                todo &= ~rdy;
            } else __builtin_amdgcn_s_sleep(1);
        }
    }

}

__global__ __launch_bounds__(768, 3) void kab(const u16* __restrict__ x, const u16* __restrict__ wqkv, const u16* __restrict__ w_r, const u16* __restrict__ gate_w,
                                          const u16* __restrict__ wg, const u16* __restrict__ wu, const u16* __restrict__ wd, const u16* __restrict__ wo,
                                          char* __restrict__ ws, u16* __restrict__ out) {
    __shared__ __attribute__((aligned(16))) char lds[AB_LDS];
    const int tid = threadIdx.x, lane = tid & 63, w = __builtin_amdgcn_readfirstlane(tid >> 6);
    const int b = blockIdx.x, xcd = b & 7, loc = b >> 3;
    const int head = xcd * 4 + (loc >> 3), chunk = loc & 7;
    if (w >= 8) { ab_loader(lds, x, wqkv, w_r, gate_w, wg, wu, wd, wo, ws, head, chunk, w - 8, lane); return; }
    const int r = lane & 31, h = lane >> 5;
    lds_cptr L = (lds_cptr)lds;
    const int rot = head & 7;
    const int blk = (lane >> 4) & 1, q4 = (lane & 15) >> 2, p4 = lane & 3;
    const int g4 = lane >> 4, t16 = lane & 15;
    unsigned xo16[2][2];
#pragma unroll
    for (int hf = 0; hf < 2; ++hf)
#pragma unroll
        for (int kk = 0; kk < 2; ++kk) { const int row = 32 * w + 16 * hf + t16; xo16[hf][kk] = row * 128 + ((4 * kk + g4) ^ ((row >> 1) & 7)) * 16; }
    const unsigned wv0 = AB_W + g4 * 256 + ((q4 * 32) ^ ((g4 & 1) * 128)) + p4 * 8, wv1 = AB_W + g4 * 256 + ((128 + q4 * 32) ^ ((g4 & 1) * 128)) + p4 * 8;
    const unsigned wa = AB_AUX + (t16 & 1) * 1024 + g4 * 16;
    const bool isrg = w == head;
    f32x4 aq0 = {}, aq1 = {}, ak0 = {}, ak1 = {}, av0 = {}, av1 = {}, ar0 = {}, ar1 = {};
    struct Frags { bf16x8 x16[2], fw[3], fa; };
    Frags fa_, fb_;
    auto rd = [&](auto RG, int i, int kk, Frags& f) {
        const int s = (rot + i) & 7;
        lds_cptr sl = L + ((i + 1) % 3) * AB_SLOT;
#pragma unroll
        for (int e = 0; e < 2; ++e) f.x16[e] = lds_rd128(sl + xo16[e][kk]);
#pragma unroll
        for (int m = 0; m < 3; ++m) {
            const s16x4 a = lds_tr(sl + wv0 + m * 2048 + kk * 1024), c = lds_tr(sl + wv1 + m * 2048 + kk * 1024);
            const s16x8 t = {a[0], a[1], a[2], a[3], c[0], c[1], c[2], c[3]};
            f.fw[m] = __builtin_bit_cast(bf16x8, t);
        }
        if constexpr (RG.value) f.fa = lds_rd128(L + wa + s * 128 + kk * 64);
        SBAR();
    };
    auto mm = [&](auto RG, Frags& f) {
        aq0 = mfma16(f.fw[0], f.x16[0], aq0); aq1 = mfma16(f.fw[0], f.x16[1], aq1);
        ak0 = mfma16(f.fw[1], f.x16[0], ak0); ak1 = mfma16(f.fw[1], f.x16[1], ak1);
        av0 = mfma16(f.x16[0], f.fw[2], av0); av1 = mfma16(f.x16[1], f.fw[2], av1);
        if constexpr (RG.value) { ar0 = mfma16(f.x16[0], f.fa, ar0); ar1 = mfma16(f.x16[1], f.fa, ar1); }
        SBAR();
    };
#define LBAR() asm volatile("s_waitcnt lgkmcnt(0)\n\ts_barrier" ::: "memory")
    { const unsigned o1 = (tid & 31) < 2 ? 0x3F803F80u : 0u; if (tid < 352) *(LDSP u32x4*)((lds_ptr)lds + AB_ONES + tid * 16) = u32x4{o1, o1, o1, o1}; }
    if (tid < 32) *(LDSP unsigned*)((lds_ptr)lds + AB_FC + tid * 4) = 0u;
    *(LDSP u16*)((lds_ptr)lds + AB_AUX + tid * 2) = w_r[tid];
    *(LDSP u16*)((lds_ptr)lds + AB_AUX + 1024 + tid * 2) = gate_w[tid];
    LBAR();
    auto phase_a = [&](auto RG) {
        rd(RG, 0, 0, fa_);
#define A_STAGE(i) rd(RG, i, 1, fb_); mm(RG, fa_); if ((i) < 7) { LBAR(); rd(RG, (i) + 1, 0, fa_); } mm(RG, fb_);
        A_STAGE(0) A_STAGE(1) A_STAGE(2) A_STAGE(3) A_STAGE(4) A_STAGE(5) A_STAGE(6) A_STAGE(7)
#undef A_STAGE
    };
    if (isrg) phase_a(BoolC<true>{}); else phase_a(BoolC<false>{});

    auto frag_qk = [&](const f32x4& a0, const f32x4& a1, float sc) {
        auto s0_ = __builtin_amdgcn_permlane16_swap(cvtpk(a0[0] * sc, a0[1] * sc), cvtpk(a1[0] * sc, a1[1] * sc), false, false);
        auto s1_ = __builtin_amdgcn_permlane16_swap(cvtpk(a0[2] * sc, a0[3] * sc), cvtpk(a1[2] * sc, a1[3] * sc), false, false);
        return u32x4{s0_[0], s1_[0], s0_[1], s1_[1]};
    };
    const u32x4 kw = frag_qk(ak0, ak1, 1.f);
    u32x4 vw;
    {
        auto s0_ = __builtin_amdgcn_permlane32_swap(cvtpk(av0[0], av0[1]), cvtpk(av1[0], av1[1]), false, false);
        auto s1_ = __builtin_amdgcn_permlane32_swap(cvtpk(av0[2], av0[3]), cvtpk(av1[2], av1[3]), false, false);
        vw = u32x4{s0_[0], s1_[0], s0_[1], s1_[1]};
    }
    const bool isv = r < 16;
    const unsigned vpos = h * 512 + blk * 256 + swap23(t16) * 16;
    st16_dev(lane * 16, kw, ws + WS_K + (size_t)head * 65536 + (8 * chunk + w) * 1024);
    st16_dev(vpos, vw, ws + WS_V + (size_t)head * 65536 + (8 * chunk + w) * 1024);
    *(LDSP u32x4*)((lds_ptr)lds + AB_K + w * 1024 + lane * 16) = kw;
    *(LDSP u32x4*)((lds_ptr)lds + AB_V + w * 1024 + vpos) = vw;
    const bf16x8 qf = __builtin_bit_cast(bf16x8, frag_qk(aq0, aq1, QSCALE));
    f32x16 zero = {};
    const float mref = max16(MFMA32(__builtin_bit_cast(bf16x8, kw), qf, zero));
    f32x16 cneg;
#pragma unroll
    for (int i = 0; i < 16; ++i) cneg[i] = -mref;

    const int tp = 4 * xcd + (loc & 3), ct = loc >> 2;
    const int tg = w & 3, cg = w >> 2;
    const int token = 64 * tp + 16 * tg + (lane & 15), col = 64 * ct + 32 * cg + 8 * (lane >> 4);
    const u32x4 xv = *(const u32x4*)(x + token * DM + col);
    LBAR();

    lds_cptr kp = L + AB_K + lane * 16;
    lds_cptr vp = isv ? L + AB_V + h * 256 + r * 16 : L + AB_ONES + (r == 16 ? 0 : r == 28 ? 16 : (r >= 20 && r < 28) ? 64 : 32);
    const unsigned vinc = isv ? 4096u : 0u;
    const u64 tag = *(LDSP const u64*)((lds_ptr)lds + AB_TAG);
    const bf16x8 kfirst = lds_rd128(kp);
    bf16x8 kf = lds_rd128(kp + 1024);
    bf16x8 v0 = lds_rd128(vp), v1 = lds_rd128(vp + 512);
    asm volatile("s_waitcnt vmcnt(0)" ::: "memory");
    if (lane == 0) st_dev(ws + WS_FLAG_A + (head * 64 + 8 * chunk + w) * 64, tag);
    f32x16 s0 = MFMA32(kfirst, qf, cneg);
    f32x16 o = {}, s1;
    u32x4 pa, pb = {0u, 0u, 0u, 0u};
#define EX(S, i) S[i] = __builtin_amdgcn_exp2f(S[i]); SBAR();
#define CV(P, S, i) P[i & 3] = cvtpk(S[2 * (i)], S[2 * (i) + 1]); SBAR();
#define STEP(SC, SN, u) \
        SN = MFMA32(kf, qf, cneg); SBAR(); \
        kf = lds_rd128(kq + ((u) + 2) * 1024); SBAR(); \
        EX(SC, 0) EX(SC, 1) EX(SC, 2) EX(SC, 3) \
        o = MFMA32(v1, __builtin_bit_cast(bf16x8, pb), o); SBAR(); \
        v1 = lds_rd128(vp + (u) * 1024 + 512); SBAR(); \
        CV(pa, SC, 0) EX(SC, 4) EX(SC, 5) CV(pa, SC, 1) EX(SC, 6) EX(SC, 7) CV(pa, SC, 2) EX(SC, 8) EX(SC, 9) CV(pa, SC, 3) EX(SC, 10) EX(SC, 11) \
        o = MFMA32(v0, __builtin_bit_cast(bf16x8, pa), o); SBAR(); \
        v0 = lds_rd128(vp + ((u) + 1) * 1024); SBAR(); \
        CV(pb, SC, 4) EX(SC, 12) EX(SC, 13) CV(pb, SC, 5) EX(SC, 14) EX(SC, 15) CV(pb, SC, 6) CV(pb, SC, 7)
    lds_cptr kq = kp;
    auto wait_words = [&](int off, unsigned p) -> unsigned {
        unsigned have = 0;
#pragma unroll 1
        for (unsigned spin = 0; spin < (1u << 20); ++spin) {
            const u32x4 v = *(LDSP volatile const u32x4*)((lds_ptr)lds + off);
            have = __builtin_amdgcn_readfirstlane(min(min(v[0], v[1]), min(v[2], v[3])));
            if (have >= p) break;
            __builtin_amdgcn_s_sleep(1);
        }
        asm volatile("" ::: "memory");
        return have;
    };
    unsigned kv_have = 0;
    auto landed = [&](unsigned p) { if (kv_have < p) kv_have = wait_words(AB_LAND, p); };
    if (isrg) {
        LDSP float* rg = (LDSP float*)((lds_ptr)lds + AB_RG);
        if (t16 < 2) { *(LDSP f32x4*)(rg + t16 * 32 + 4 * g4) = ar0; *(LDSP f32x4*)(rg + t16 * 32 + 16 + 4 * g4) = ar1; }
        const int token = 256 * chunk + 32 * w + r;
        const unsigned rb = cvtpk(rg[r], 0.f) & 0xFFFFu;
        unsigned key = (((rb & 0x8000u) ? (~rb & 0xFFFFu) : (rb | 0x8000u)) << 16) | (0xFFFFu - token);
#pragma unroll
        for (int o = 16; o >= 1; o >>= 1) { const unsigned t = __shfl_xor(key, o); key = t > key ? t : key; }
        if (lane == 0) st_dev(ws + WS_KEYS + (8 * chunk + w) * 8, (tag << 32) | key);
        const float gs = rbf(rg[32 + r]);
        const float g = rbf(1.f / (1.f + __expf(-gs)));
        if (lane < 32) st_dev(ws + WS_GATE + token * 8, (tag << 32) | __float_as_uint(g));
    }

    constexpr int PRIO_MID = 5;
    if (w < 4) __builtin_amdgcn_s_setprio(0); else __builtin_amdgcn_s_setprio(1);
    const int prio_at = w < 4 ? PRIO_MID : -1;
    unsigned special = 0x2AAC;
#pragma unroll 1
    for (int m = 0; m < 16; ++m) {
        if (__builtin_expect((special >> m) & 1, 0)) {
            if (m == 2) {

                landed(1);

                s0 = MFMA32(lds_rd128(kq), qf, cneg); kf = lds_rd128(kq + 1024); v0 = lds_rd128(vp);
            } else if (m & 1) landed((m + 1) >> 1);
            if (kv_have >= 7) special = 0;
        }
        SBAR();
        STEP(s0, s1, 0) STEP(s1, s0, 1)
        asm volatile("s_cmp_eq_u32 %0, %1\n\ts_cbranch_scc0 1f\n\ts_setprio 2\n1:" ::"s"(m), "s"(prio_at) : "scc");
        SBAR();
        STEP(s0, s1, 2) STEP(s1, s0, 3)
        kq += 4096; vp += vinc;
    }
#undef STEP
#undef EX
#undef CV
    o = MFMA32(v1, __builtin_bit_cast(bf16x8, pb), o);

    float l = h ? o[12] : o[8];
    float chk = 0.f;
#pragma unroll
    for (int i = 0; i < 8; ++i) chk += fabsf(o[i]);
    const bool bad = !(l < 1.152921504606847e18f) || !(chk < 3.0e38f);
    if (__any(bad)) {
        float m = -INFINITY;
        o = f32x16{};
        lds_cptr vq = isv ? L + AB_V + h * 256 + r * 16 : L + AB_ONES + (r == 16 ? 0 : r == 28 ? 16 : 32);
        for (int t = 0; t < 64; ++t) {
            f32x16 s = MFMA32(lds_rd128(kp + t * 1024), qf, zero);
            const float mn = fmaxf(m, max16(s));
            const float f = __builtin_amdgcn_exp2f(m - mn);
            m = mn;
#pragma unroll
            for (int i = 0; i < 16; ++i) { s[i] -= mn; o[i] *= f; }
            bf16x8 p0, p1;
            exp_pack(s, p0, p1);
            lds_cptr vt = vq + (isv ? t * 1024 : 0);
            o = MFMA32(lds_rd128(vt), p0, o); o = MFMA32(lds_rd128(vt + 512), p1, o);
        }
        l = h ? o[12] : o[8];
    }
    const float inv = 1.f / l;
    u32x4 ov;
#pragma unroll
    for (int i = 0; i < 4; ++i) ov[i] = cvtpk(o[2 * i] * inv, o[2 * i + 1] * inv);
    st16_dev(lane * 16, ov, ws + WS_O + (size_t)((8 * chunk + w) * 32 + head) * 1024);
    asm volatile("s_waitcnt lgkmcnt(0)" ::: "memory");
    if (lane == 0) __hip_atomic_fetch_add((LDSP unsigned*)((lds_ptr)lds + AB_KVDONE), 1u, __ATOMIC_RELAXED, __HIP_MEMORY_SCOPE_WORKGROUP);
    __builtin_amdgcn_s_setprio(0);
    asm volatile("s_waitcnt vmcnt(0)" ::: "memory");
    if (lane == 0) st_dev(ws + WS_FLAG_O + (head * 64 + 8 * chunk + w) * 64, tag);

    const unsigned wq3 = 8192 + cg * 4096 + (8 * g4 + q4) * 64 + p4 * 16;
    const unsigned oo3 = (tg >> 1) * 4096 + (g4 >> 1) * 1024 + (g4 & 1) * 512 + (16 * (tg & 1) + (lane & 15)) * 16;
    f32x4 ac0 = {}, ac1 = {};
    u32x4 y0, y1; unsigned idxb;
    lds_wait((LDSP unsigned*)((lds_ptr)lds + AB_SEEN), 1);
    asm volatile("global_load_dwordx4 %0, %3, %4 sc1\n\tglobal_load_dwordx4 %1, %3, %4 offset:16 sc1\n\tglobal_load_dword %2, %5, %4 offset:2048 sc1"
                 : "=&v"(y0), "=&v"(y1), "=&v"(idxb) : "v"(col * 4), "s"(ws + WS_Y2), "v"(ct * 4) : "memory");
    unsigned gtb = 0;
#pragma unroll 1
    for (unsigned spin = 0; spin < (1u << 20); ++spin) {
        const u64 gv = ld_dev(ws + WS_GATE + token * 8);
        gtb = (unsigned)gv;
        if (__all((unsigned)(gv >> 32) == (unsigned)tag)) break;
        __builtin_amdgcn_s_sleep(2);
    }
    asm volatile("" : "+v"(y0), "+v"(y1), "+v"(idxb));
    {
        LDSP volatile const unsigned* lw = (LDSP volatile const unsigned*)((lds_ptr)lds + AB_LANDS) + (lane < 12 ? lane : 0);
        unsigned have = 0;
        auto need = [&](unsigned bits) {
            if ((have & bits) == bits) return;
#pragma unroll 1
            for (unsigned spin = 0; spin < (1u << 20); ++spin) {
                have = (unsigned)__ballot(*lw != 0u);
                if ((have & bits) == bits) break;
                __builtin_amdgcn_s_sleep(1);
            }
            asm volatile("" ::: "memory");
        };
        bf16x8 fa[6], fb[6];
        auto rd = [&](int s, bf16x8 (&f)[6]) {
            lds_cptr sb = L + s * 16384;
#pragma unroll
            for (int k2 = 0; k2 < 2; ++k2) { f[3 * k2] = lds_rd128(sb + oo3 + k2 * 2048); f[3 * k2 + 1] = lds_tr2(sb + wq3 + k2 * 2048); f[3 * k2 + 2] = lds_tr2(sb + wq3 + k2 * 2048 + 8); }
            SBAR();
        };
        auto mm = [&](bf16x8 (&f)[6]) {
#pragma unroll
            for (int k2 = 0; k2 < 2; ++k2) { ac0 = mfma16(f[3 * k2 + 1], f[3 * k2], ac0); ac1 = mfma16(f[3 * k2 + 2], f[3 * k2], ac1); }
            SBAR();
        };
        need(0xF01); rd(0, fa);
        need(0x002); rd(1, fb); mm(fa);
        need(0x004); rd(2, fa); mm(fb);
        need(0x008); rd(3, fb); mm(fa);
        need(0x010); rd(4, fa); mm(fb);
        need(0x020); rd(5, fb); mm(fa);
        need(0x040); rd(6, fa); mm(fb);
        need(0x080); rd(7, fb); mm(fa); mm(fb);
    }

    asm volatile("s_waitcnt vmcnt(0)" : "+v"(y0), "+v"(y1), "+v"(idxb)::"memory");
    {
        const bool sel = token == (int)idxb;
        const float gt = __uint_as_float(gtb), omg = 1.f - gt;
        u32x4 res;
#pragma unroll
        for (int i = 0; i < 4; ++i) {
            float o2[2];
#pragma unroll
            for (int e = 0; e < 2; ++e) {
                const int c = 2 * i + e;
                const float a = c < 4 ? ac0[c & 3] : ac1[c & 3];
                const float xx = e ? __uint_as_float(xv[i] & 0xFFFF0000u) : __uint_as_float(xv[i] << 16);
                const float ys = __uint_as_float(c < 4 ? y0[c & 3] : y1[c & 3]);
                o2[e] = gt * a + omg * (xx + (sel ? ys : 0.f));
            }
            res[i] = cvtpk(o2[0], o2[1]);
        }
        st16_dev((token * DM + col) * 2, res, out);
    }

}

extern "C" void kernel_launch(void* const* d_in, const int* in_sizes, int n_in, void* d_out, int out_size, void* d_ws, size_t ws_size, hipStream_t stream) {
    const u16* x = (const u16*)d_in[0];
    const u16* wqkv = (const u16*)d_in[1];
    const u16* wo = (const u16*)d_in[2];
    const u16* w_r = (const u16*)d_in[4];
    const u16* wg = (const u16*)d_in[5];
    const u16* wu = (const u16*)d_in[6];
    const u16* wd = (const u16*)d_in[7];
    const u16* gate_w = (const u16*)d_in[8];
    char* ws = (char*)d_ws;
    kab<<<256, 768, 0, stream>>>(x, wqkv, w_r, gate_w, wg, wu, wd, wo, ws, (u16*)d_out);
}
```
